```python
import torch

def _softmax(x, axis=-1):
    return torch.softmax(x, dim=axis)
def _gelu(x):
    return torch.nn.functional.gelu(x, approximate="tanh")
def _silu(x):
    return torch.nn.functional.silu(x)
def _sigmoid(x):
    return torch.sigmoid(x)
def _layernorm(x, axis=-1, eps=1e-5):
    m = x.mean(dim=axis, keepdim=True)
    v = x.var(dim=axis, keepdim=True, unbiased=False)
    return (x - m) / torch.sqrt(v + eps)
def _scatter_add(tgt, idx, src, axis=0):
    return tgt.clone().index_add_(axis, idx, src)
def _segment_sum(src, seg, n):
    out = torch.zeros((n,) + src.shape[1:], dtype=src.dtype)
    return out.index_add_(0, seg, src)
def _pad(x, pw):
    flat = tuple(v for pair in reversed(pw) for v in pair)
    return torch.nn.functional.pad(x, flat)

kernel_name = 'recurrent_scan__ssm_conv_s4d__geglu_ffn__prompt'

def setup_inputs():

    _w = (lambda a: (lambda t: t.to(torch.bfloat16) if t.is_floating_point() else t)(torch.as_tensor(a)))
    def _A_setup():
        Bsz, L, D, N = 1, 2048, 512, 128
        return {
            "u": _w((lambda *s: torch.randn(*s, dtype=torch.bfloat16) * 0.05)(Bsz, L, D)),
            "log_A_real": _w((lambda *s: torch.randn(*s, dtype=torch.bfloat16) * 6.4)(D, N) * 0.5),
            "A_imag": _w((lambda *s: torch.randn(*s, dtype=torch.bfloat16) * 0.05)(D, N) * 3.14),
            "C_real": _w((lambda *s: torch.randn(*s, dtype=torch.bfloat16) * 0.05)(D, N) / N ** 0.5),
            "C_imag": _w((lambda *s: torch.randn(*s, dtype=torch.bfloat16) * 0.05)(D, N) / N ** 0.5),
            "log_dt": _w((lambda *s: torch.randn(*s, dtype=torch.bfloat16) * 0.05)(D) * 0.5 - 4.0),
            "D_skip": _w((lambda *s: torch.randn(*s, dtype=torch.bfloat16) * 0.05)(D)),
        }
    _A_in = _A_setup()

    def _B_setup():
        B, S, D, F = 1, 2048, 512, 1792
        return {
            "x": _w((lambda *s: torch.randn(*s, dtype=torch.bfloat16) * 0.05)(B, S, D)),
            "w_gate": _w((lambda *s: torch.randn(*s, dtype=torch.bfloat16) * 0.05)(D, F)),
            "w_up": _w((lambda *s: torch.randn(*s, dtype=torch.bfloat16) * 0.05)(D, F)),
            "w_down": _w((lambda *s: torch.randn(*s, dtype=torch.bfloat16) * 0.05)(F, D)),
        }
    _B_in = _B_setup()

    return {
        "A_u": _A_in["u"],
        "A_log_A_real": _A_in["log_A_real"],
        "A_A_imag": _A_in["A_imag"],
        "A_C_real": _A_in["C_real"],
        "A_C_imag": _A_in["C_imag"],
        "A_log_dt": _A_in["log_dt"],
        "A_D_skip": _A_in["D_skip"],
        "B_x": _B_in["x"],
        "B_w_gate": _B_in["w_gate"],
        "B_w_up": _B_in["w_up"],
        "B_w_down": _B_in["w_down"],
    }

def reference(A_u, A_log_A_real, A_A_imag, A_C_real, A_C_imag, A_log_dt, A_D_skip, B_x, B_w_gate, B_w_up, B_w_down):
    def _A_ref(u, log_A_real, A_imag, C_real, C_imag, log_dt, D_skip):
        L = u.shape[1]
        dt = torch.exp(log_dt)
        A = -torch.exp(log_A_real) + 1j * A_imag
        C = (C_real + 1j * C_imag) * dt[:, None]
        pos = (lambda x, dt: x.to(dt))(torch.arange(L), log_dt.dtype)
        P = torch.exp((A * dt[:, None])[:, :, None] * pos[None, None, :])
        K = 2.0 * torch.einsum("dn,dnl->dl", C, P).real
        L2 = 2 * L
        u_t = (lambda x, *d: x.permute(*d))(u, 0, 2, 1)
        Kf = (lambda x, *a, **k: torch.fft.rfft(x.float(), *a, **k))(K, L2)
        Uf = (lambda x, *a, **k: torch.fft.rfft(x.float(), *a, **k))(u_t, L2)
        y = (lambda x, *a, **k: torch.fft.irfft(x, *a, **k).to(torch.bfloat16))(Uf * Kf[None, :, :], L2)[:, :, :L]
        y = (lambda x, *d: x.permute(*d))(y, 0, 2, 1)
        return y + u * D_skip[None, None, :]

    def _B_ref(x, w_gate, w_up, w_down):
        return (_gelu(x @ w_gate) * (x @ w_up)) @ w_down

    x = A_u
    xn = _layernorm(x, axis=-1)
    s = _A_ref(xn, A_log_A_real, A_A_imag, A_C_real, A_C_imag, A_log_dt, A_D_skip)
    g = _silu(_B_ref(xn, B_w_gate, B_w_up, B_w_down))
    return x + s * g
```

```cpp
#include <hip/hip_runtime.h>
#include <hip/hip_bf16.h>
#include <stdint.h>

typedef unsigned short u16;
typedef __attribute__((ext_vector_type(4))) short s16x4;
typedef __attribute__((ext_vector_type(8))) __bf16 bf16x8;
typedef __attribute__((ext_vector_type(8))) _Float16 f16x8;
typedef __attribute__((ext_vector_type(2))) _Float16 f16x2;
typedef __attribute__((ext_vector_type(2))) __fp16 h16x2;
typedef __attribute__((ext_vector_type(4))) float f32x4;
typedef __attribute__((ext_vector_type(16))) float f32x16;
typedef __attribute__((ext_vector_type(4))) unsigned u32x4;
typedef __attribute__((ext_vector_type(2))) unsigned u32x2;
#define LAS __attribute__((address_space(3)))
#define GAS __attribute__((address_space(1)))

constexpr int L_ = 2048, D_ = 512, N_ = 128, F_ = 1792;
constexpr int KR_ = 2240;
constexpr int KZ_ = 2080;

constexpr size_t OFF_XN = 0;
constexpr size_t OFF_UT = 2u << 20;
constexpr size_t OFF_KR = 4u << 20;
constexpr size_t OFF_KINV = 7u << 20;
constexpr size_t OFF_CNT = (7u << 20) + 65536;
constexpr size_t OFF_H = 8u << 20;
constexpr size_t OFF_ST = 16u << 20;

__device__ __forceinline__ float bf2f(unsigned v) { return __uint_as_float(v << 16); }
__device__ __forceinline__ float bflo(unsigned v) { return __uint_as_float(v << 16); }
__device__ __forceinline__ float bfhi(unsigned v) { return __uint_as_float(v & 0xffff0000u); }
__device__ __forceinline__ float bfr(float f) { return (float)(__bf16)f; }
__device__ __forceinline__ unsigned pkbf(float a, float b) {
  typedef __attribute__((ext_vector_type(2))) __bf16 bf16x2;
  bf16x2 v; v.x = (__bf16)a; v.y = (__bf16)b;
  return __builtin_bit_cast(unsigned, v);
}
__device__ __forceinline__ unsigned pkh(float a, float b) {
  f16x2 v; v.x = (_Float16)a; v.y = (_Float16)b;
  return __builtin_bit_cast(unsigned, v);
}
__device__ __forceinline__ void glds16(const void* g, LAS void* l) {
  __builtin_amdgcn_global_load_lds((const GAS void*)g, l, 16, 0, 0);
}
__device__ __forceinline__ s16x4 trread(LAS char* p) {
  return __builtin_amdgcn_ds_read_tr16_b64_v4i16((LAS s16x4*)p);
}
union Frag { struct { s16x4 a, b; } s; bf16x8 bf; f16x8 h; u32x4 u; };

#define WAIT_VM(n) asm volatile("s_waitcnt vmcnt(" #n ")" ::: "memory")
#define BARRIER() do { asm volatile("s_waitcnt lgkmcnt(0)" ::: "memory"); __builtin_amdgcn_s_barrier(); asm volatile("" ::: "memory"); } while (0)

__device__ __forceinline__ void cexp_p(float arl2, float ai, float p, float& re, float& im) {
  const float INV2PI_HI = 0.15915494309189535f;
  const float INV2PI_LO = 6.4206382e-09f;
  float mag = __builtin_amdgcn_exp2f(arl2 * p);
  float t = ai * p;
  float rh = t * INV2PI_HI;
  float rl = fmaf(t, INV2PI_HI, -rh);
  rl = fmaf(t, INV2PI_LO, rl);
  float fr = __builtin_amdgcn_fractf(rh) + rl;
  re = mag * __builtin_amdgcn_cosf(fr);
  im = mag * __builtin_amdgcn_sinf(fr);
}
__device__ __forceinline__ void split_h(float a, float b, unsigned& hi, unsigned& lo) {
  h16x2 h = __builtin_amdgcn_cvt_pkrtz(a, b);
  float ra = a - (float)h.x, rb = b - (float)h.y;
  h16x2 l = __builtin_amdgcn_cvt_pkrtz(ra, rb);
  hi = __builtin_bit_cast(unsigned, h);
  lo = __builtin_bit_cast(unsigned, l);
}

constexpr int K0_LN_BLOCKS = 64;
__global__ __launch_bounds__(256) void k0_prep(const u16* __restrict__ x, const u16* __restrict__ lAr,
                                               const u16* __restrict__ Aim, const u16* __restrict__ Cr,
                                               const u16* __restrict__ Ci, const u16* __restrict__ ldt,
                                               u16* __restrict__ xn, u16* __restrict__ uT, u16* __restrict__ Kr,
                                               float* __restrict__ kinv, unsigned* __restrict__ cnt) {
  __shared__ __attribute__((aligned(16))) unsigned sm[32 * 257 + 16];
  const int tid = threadIdx.x, w = tid >> 6, lane = tid & 63;
  if (blockIdx.x < K0_LN_BLOCKS) {
    if (blockIdx.x == 0 && tid < 8) cnt[tid * 64] = 0u;
    const int row0 = blockIdx.x * 32 + w * 8;
    u32x4 v[8];
#pragma unroll
    for (int r = 0; r < 8; ++r) v[r] = *(const u32x4*)(x + (size_t)(row0 + r) * D_ + lane * 8);
    float f[8][8], s[8];
#pragma unroll
    for (int r = 0; r < 8; ++r) {
#pragma unroll
      for (int j = 0; j < 4; ++j) { f[r][2 * j] = bflo(v[r][j]); f[r][2 * j + 1] = bfhi(v[r][j]); }
      s[r] = ((f[r][0] + f[r][1]) + (f[r][2] + f[r][3])) + ((f[r][4] + f[r][5]) + (f[r][6] + f[r][7]));
    }
#pragma unroll
    for (int o = 1; o < 64; o <<= 1)
#pragma unroll
      for (int r = 0; r < 8; ++r) s[r] += __shfl_xor(s[r], o);
    float q[8];
#pragma unroll
    for (int r = 0; r < 8; ++r) {
      s[r] *= (1.0f / D_);
      float a = 0.f;
#pragma unroll
      for (int j = 0; j < 8; ++j) { float d = f[r][j] - s[r]; a = fmaf(d, d, a); }
      q[r] = a;
    }
#pragma unroll
    for (int o = 1; o < 64; o <<= 1)
#pragma unroll
      for (int r = 0; r < 8; ++r) q[r] += __shfl_xor(q[r], o);
#pragma unroll
    for (int r = 0; r < 8; ++r) {
      const float mb = bfr(s[r]);
      const float vb = bfr(q[r] * (1.0f / D_));
      const float sd = bfr(sqrtf(bfr(vb + 1e-5f)));
      float o[8];
#pragma unroll
      for (int j = 0; j < 8; ++j) o[j] = bfr(bfr(f[r][j] - mb) / sd);
      u32x4 pb, ph;
#pragma unroll
      for (int j = 0; j < 4; ++j) { pb[j] = pkbf(o[2 * j], o[2 * j + 1]); ph[j] = pkh(o[2 * j], o[2 * j + 1]); }
      *(u32x4*)(xn + (size_t)(row0 + r) * D_ + lane * 8) = pb;
      unsigned* dst = sm + (w * 8 + r) * 257 + lane * 4;
#pragma unroll
      for (int j = 0; j < 4; ++j) dst[j] = ph[j];
    }
    __syncthreads();
    unsigned c[32];
#pragma unroll
    for (int r = 0; r < 32; ++r) c[r] = sm[r * 257 + tid];
    u16* d0 = uT + (size_t)(2 * tid) * L_ + blockIdx.x * 32;
#pragma unroll
    for (int k = 0; k < 4; ++k) {
      u32x4 e, o;
#pragma unroll
      for (int j = 0; j < 4; ++j) {
        unsigned a = c[8 * k + 2 * j], b = c[8 * k + 2 * j + 1];
        e[j] = (a & 0xffffu) | (b << 16);
        o[j] = (a >> 16) | (b & 0xffff0000u);
      }
      *(u32x4*)(d0 + 8 * k) = e;
      *(u32x4*)(d0 + L_ + 8 * k) = o;
    }
    return;
  }
  const int d = blockIdx.x - K0_LN_BLOCKS;
  const float dt = bfr(expf(bf2f(ldt[d])));
  float sb;
  {
    unsigned a = *(const unsigned*)(Cr + d * N_ + 2 * lane), b = *(const unsigned*)(Ci + d * N_ + 2 * lane);
    sb = (fabsf(bflo(a)) + fabsf(bfhi(a))) + (fabsf(bflo(b)) + fabsf(bfhi(b)));
#pragma unroll
    for (int o = 1; o < 64; o <<= 1) sb += __shfl_xor(sb, o);
    sb = sb * dt * 2.0f;
  }
  int ex = (int)((__float_as_uint(sb) >> 23) & 0xff) - 127;
  int se = 14 - ex;
  se = se > 100 ? 100 : (se < -100 ? -100 : se);
  const float scale = __uint_as_float((unsigned)(127 + se) << 23);
  const float cs = dt * 2.0f * scale;
  const int row = lane & 31, hh = lane >> 5;
  int prow;
  if (row < 8) prow = row;
  else if (row < 12) prow = 256 + 2 * (row - 8);
  else if (row < 16) prow = 512 + 256 * ((row - 12) >> 1) + 4 * ((row - 12) & 1);
  else prow = 1024 + 256 * (row - 16);
  const float pA = (float)prow, pB = (float)(8 * row);
  const float amask = row < 21 ? 1.0f : 0.0f;
  f32x16 acc = {};
#pragma unroll
  for (int ks = 0; ks < 4; ++ks) {
    const int n0 = d * N_ + w * 32 + ks * 8 + hh * 4;
    const u32x2 v0 = *(const u32x2*)(lAr + n0), v1 = *(const u32x2*)(Aim + n0);
    const u32x2 v2 = *(const u32x2*)(Cr + n0), v3 = *(const u32x2*)(Ci + n0);
    Frag Ah, Al, Bh, Bl;
#pragma unroll
    for (int q = 0; q < 4; ++q) {
      const float la = (q & 1) ? bfhi(v0[q >> 1]) : bflo(v0[q >> 1]);
      const float im = (q & 1) ? bfhi(v1[q >> 1]) : bflo(v1[q >> 1]);
      const float cr = ((q & 1) ? bfhi(v2[q >> 1]) : bflo(v2[q >> 1])) * cs * amask;
      const float ci = ((q & 1) ? bfhi(v3[q >> 1]) : bflo(v3[q >> 1])) * cs * amask;
      const float ar = -bfr(expf(la)) * dt;
      const float arl2 = ar * 1.4426950408889634f;
      const float ai = im * dt;
      float er, ei, yr, yi;
      cexp_p(arl2, ai, pA, er, ei);
      cexp_p(arl2, ai, pB, yr, yi);
      const float xr = cr * er - ci * ei, xi = cr * ei + ci * er;
      unsigned h0, l0, h1, l1;
      split_h(xr, xi, h0, l0);
      split_h(yr, -yi, h1, l1);
      Ah.u[q] = h0; Al.u[q] = l0; Bh.u[q] = h1; Bl.u[q] = l1;
    }
    acc = __builtin_amdgcn_mfma_f32_32x32x16_f16(Al.h, Bh.h, acc, 0, 0, 0);
    acc = __builtin_amdgcn_mfma_f32_32x32x16_f16(Ah.h, Bl.h, acc, 0, 0, 0);
    acc = __builtin_amdgcn_mfma_f32_32x32x16_f16(Ah.h, Bh.h, acc, 0, 0, 0);
  }
  float* part = (float*)sm;
#pragma unroll
  for (int i = 0; i < 16; ++i) {
    const int rr = (i & 3) + 8 * (i >> 2) + 4 * hh;
    part[w * 1024 + rr * 32 + row] = acc[i];
  }
  __syncthreads();
  float* tab = (float*)sm + 4096;
  for (int e = tid; e < 21 * 32; e += 256) tab[e] = (part[e] + part[1024 + e]) + (part[2048 + e] + part[3072 + e]);
  __syncthreads();
  for (int c = tid; c < KR_ / 8; c += 256) {
    float v[8];
#pragma unroll
    for (int j = 0; j < 8; ++j) {
      const int idx = KZ_ - (8 * c + j);
      float val = 0.f;
      if (idx >= 0 && idx < L_) {
        unsigned u = __float_as_uint((float)idx);
        u = (u + 0x7fffu + ((u >> 16) & 1u)) & 0xffff0000u;
        const int p = (int)__uint_as_float(u);
        const int r = p & 7, cc = p >> 3, c1 = cc >> 5, c0 = cc & 31;
        int rw;
        if (c1 == 0) rw = r;
        else if (c1 == 1) rw = 8 + (r >> 1);
        else if (c1 < 4) rw = 12 + 2 * (c1 - 2) + (r >> 2);
        else rw = 16 + (c1 - 4);
        val = tab[rw * 32 + c0];
      }
      v[j] = val;
    }
    u32x4 o;
#pragma unroll
    for (int j = 0; j < 4; ++j) o[j] = pkh(v[2 * j], v[2 * j + 1]);
    *(u32x4*)(Kr + (size_t)d * KR_ + 8 * c) = o;
  }
  if (tid == 0) kinv[d] = __uint_as_float((unsigned)(127 - se) << 23);
}

constexpr int G1_A = 16384, G1_W = 28672, G1_STAGE = G1_A + G1_W, G1_NS = 3;
constexpr int G1_LDS = G1_STAGE * G1_NS;

__device__ __forceinline__ float gelu_t(float x) {
  const float c1 = -2.0f * 0.7978845608028654f * 1.4426950408889634f;
  const float c2 = c1 * 0.044715f;
  float a = x * fmaf(x * x, c2, c1);
  float e = __builtin_amdgcn_exp2f(a);
  return x * __builtin_amdgcn_rcpf(1.0f + e);
}

__global__ __launch_bounds__(256, 1) void k1_gemm1(const u16* __restrict__ xn, const u16* __restrict__ wg,
                                                   const u16* __restrict__ wu, u16* __restrict__ h) {
  extern __shared__ __attribute__((aligned(16))) char smem_[];
  LAS char* smem = (LAS char*)smem_;
  const int tid = threadIdx.x, lane = tid & 63;
  const int w = __builtin_amdgcn_readfirstlane(tid >> 6);
  const int b = blockIdx.x, xcd = b & 7, idx = b >> 3;
  const int mt = (xcd & 1) * 8 + (idx & 7), nt = (xcd >> 1) * 4 + (idx >> 3);
  const int m0 = mt * 128, n0 = nt * 112;

  const u16* asrc[4];
#pragma unroll
  for (int i = 0; i < 4; ++i) {
    const int row = 8 * (4 * w + i) + (lane >> 3);
    const int ch = (lane & 7) ^ ((row >> 1) & 7);
    asrc[i] = xn + (size_t)(m0 + row) * D_ + 8 * ch;
  }
  const u16* wsrc[7];
#pragma unroll
  for (int i = 0; i < 7; ++i) {
    const int sid = 4 * (7 * w + i) + (lane >> 4);
    const int km = sid / 7, cb = sid - km * 7, kb = km >> 1, mat = km & 1;
    const int k = 8 * kb + ((((lane & 15) >> 1)) ^ (4 * mat));
    wsrc[i] = (mat ? wu : wg) + (size_t)k * F_ + n0 + 16 * cb + 8 * (lane & 1);
  }
  auto stage = [&](int s) {
    LAS char* base = smem + (s % G1_NS) * G1_STAGE;
#pragma unroll
    for (int i = 0; i < 4; ++i) glds16(asrc[i] + s * 64, base + (4 * w + i) * 1024);
#pragma unroll
    for (int i = 0; i < 7; ++i) glds16(wsrc[i] + (size_t)s * 64 * F_, base + G1_A + (7 * w + i) * 1024);
  };
  const int mrow = 32 * w + (lane & 31), hh = lane >> 5;
  int xoff[4];
#pragma unroll
  for (int ks = 0; ks < 4; ++ks) xoff[ks] = mrow * 128 + (((2 * ks + hh) ^ ((mrow >> 1) & 7)) << 4);
  const int mat = (lane >> 4) & 1, q = (lane & 15) >> 2, p = lane & 3;
  const int pp = ((p & 1) << 1) | (p >> 1);
  const int woff0 = G1_A + ((hh * 2 + mat) * 7) * 256 + (4 * mat + q) * 32 + pp * 8;
  const int woff1 = G1_A + ((hh * 2 + mat) * 7) * 256 + (4 * (1 - mat) + q) * 32 + pp * 8;

  f32x16 acc[7];
#pragma unroll
  for (int t = 0; t < 7; ++t) acc[t] = (f32x16){};

  stage(0); stage(1);
#pragma unroll 1
  for (int s = 0; s < 8; ++s) {
    if (s < 7) WAIT_VM(11); else WAIT_VM(0);
    BARRIER();
    if (s + 2 < 8) stage(s + 2);
    LAS char* base = smem + (s % G1_NS) * G1_STAGE;
#pragma unroll
    for (int ks = 0; ks < 4; ++ks) {
      Frag xf; xf.u = *(LAS u32x4*)(base + xoff[ks]);
#pragma unroll
      for (int t = 0; t < 7; ++t) {
        Frag wf;
        wf.s.a = trread(base + woff0 + ks * 7168 + t * 256);
        wf.s.b = trread(base + woff1 + ks * 7168 + t * 256);
        acc[t] = __builtin_amdgcn_mfma_f32_32x32x16_bf16(wf.bf, xf.bf, acc[t], 0, 0, 0);
      }
    }
  }
  u16* hp = h + (size_t)(m0 + mrow) * F_ + n0 + 8 * hh;
#pragma unroll
  for (int t = 0; t < 7; ++t) {
    float o[8];
#pragma unroll
    for (int i = 0; i < 8; ++i) o[i] = bfr(gelu_t(bfr(acc[t][i]))) * bfr(acc[t][8 + i]);
    u32x4 v;
#pragma unroll
    for (int j = 0; j < 4; ++j) v[j] = pkbf(o[2 * j], o[2 * j + 1]);
    *(u32x4*)(hp + 16 * t) = v;
  }
}

constexpr int G2_STAGE = 16384, G2_NS = 5, K2_LDS = G2_STAGE * G2_NS;
constexpr int CV_KS = 4672;
constexpr int CV_US = 4736;
constexpr int CV_CH = 4 * CV_KS + 2 * CV_US;
static_assert(CV_CH % 256 == 0, "conv channel stride");

__device__ __forceinline__ void conv_role(LAS char* smem, int cb, const u16* __restrict__ uT, const u16* __restrict__ Kr,
                                          const float* __restrict__ kinv, const u16* __restrict__ dskip,
                                          u16* __restrict__ sT, unsigned* __restrict__ cnt) {
  const int tid = threadIdx.x, lane = tid & 63;
  const int w = __builtin_amdgcn_readfirstlane(tid >> 6);
  const int d0 = cb * 2;
  constexpr int KB2 = 64, KB4 = 64 + CV_KS, KB6 = 64 + 2 * CV_KS, KB0 = 64 + 3 * CV_KS - 16;
  constexpr int UB0 = 4 * CV_KS + 64, UB1 = UB0 + CV_US;
#pragma unroll
  for (int ch = 0; ch < 2; ++ch) {
    LAS char* cbase = smem + ch * CV_CH;
    const u16* kr = Kr + (size_t)(d0 + ch) * KR_;
    for (int c = tid; c < 276; c += 256) {
      const u32x4 a = *(const u32x4*)(kr + 8 * c), bb = *(const u32x4*)(kr + 8 * c + 8);
      *(LAS u32x4*)(cbase + KB0 + 16 * c) = a;
      *(LAS u32x4*)(cbase + KB2 + 16 * c) = (u32x4){a[1], a[2], a[3], bb[0]};
      *(LAS u32x4*)(cbase + KB4 + 16 * c) = (u32x4){a[2], a[3], bb[0], bb[1]};
      *(LAS u32x4*)(cbase + KB6 + 16 * c) = (u32x4){a[3], bb[0], bb[1], bb[2]};
    }
    const u16* ur = uT + (size_t)(d0 + ch) * L_;
    {
      const int c = tid;
      const u32x4 a = *(const u32x4*)(ur + 8 * c);
      const unsigned nx = (c < 255) ? (unsigned)ur[8 * c + 8] : 0u;
      const int P = c + 2 * (c >> 4) + 4;
      *(LAS u32x4*)(cbase + UB0 + 16 * P) = a;
      u32x4 s1;
      s1[0] = (a[0] >> 16) | (a[1] << 16); s1[1] = (a[1] >> 16) | (a[2] << 16);
      s1[2] = (a[2] >> 16) | (a[3] << 16); s1[3] = (a[3] >> 16) | (nx << 16);
      *(LAS u32x4*)(cbase + UB1 + 16 * P) = s1;
      if (c == 0) {
        const u32x4 z = {0u, 0u, 0u, 0u};
        *(LAS u32x4*)(cbase + UB0) = z; *(LAS u32x4*)(cbase + UB0 + 16) = z;
        *(LAS u32x4*)(cbase + UB1) = z; *(LAS u32x4*)(cbase + UB1 + 16) = (u32x4){0u, 0u, 0u, a[0] << 16};
      }
    }
  }
  __syncthreads();
  const int ch = w >> 1, wv = w & 1;
  LAS char* cbase = smem + ch * CV_CH;
  const int r = lane & 15, g = lane >> 4;
  const int e = (-2 * r) & 7;
  const int kb = (e == 0) ? KB0 : (e == 2 ? KB2 : (e == 4 ? KB4 : KB6));
  const int kl = kb + 16 * (g - ((r + 3) >> 2)) + 2 * KZ_;
  const int ubase = (r & 1) ? UB1 : UB0;
  const int ul = ubase + 16 * (18 * (r >> 1) + g);
  auto ldA = [&](int kappa) { Frag f; f.u = *(LAS u32x4*)(cbase + kl - 2 * kappa); return f; };
  auto ldB = [&](int T, int c0) {
    const int c8 = c0 >> 3;
    const int sp = c8 + 2 * (c8 >> 4) + 4 + 144 * T;
    int a = ul + 16 * sp; a = a < ubase ? ubase : a;
    Frag f; f.u = *(LAS u32x4*)(cbase + a); return f;
  };
#define MF(acc_, A_, B_) acc_ = __builtin_amdgcn_mfma_f32_16x16x32_f16(A_.h, B_.h, acc_, 0, 0, 0)
  f32x4 a0[4] = {}, a1[4] = {};
  Frag W0, W1, W2, W3;
  if (wv == 0) {
    int c0 = -1952;
    W3 = ldA(32 - c0); W2 = ldA(64 - c0); W1 = ldA(96 - c0);
#pragma unroll 1
    for (int it = 0; it < 12; ++it) {
      Frag B;
      W0 = ldA(-c0); B = ldB(1, c0); MF(a1[3], W1, B); MF(a1[2], W2, B); MF(a1[1], W3, B); MF(a1[0], W0, B); c0 += 32;
      W1 = ldA(-c0); B = ldB(1, c0); MF(a1[3], W2, B); MF(a1[2], W3, B); MF(a1[1], W0, B); MF(a1[0], W1, B); c0 += 32;
      W2 = ldA(-c0); B = ldB(1, c0); MF(a1[3], W3, B); MF(a1[2], W0, B); MF(a1[1], W1, B); MF(a1[0], W2, B); c0 += 32;
      W3 = ldA(-c0); B = ldB(1, c0); MF(a1[3], W0, B); MF(a1[2], W1, B); MF(a1[1], W2, B); MF(a1[0], W3, B); c0 += 32;
    }
  } else {
    int c0 = -928;
    W3 = ldA(32 - c0); W2 = ldA(64 - c0); W1 = ldA(96 - c0);
#pragma unroll 1
    for (int it = 0; it < 4; ++it) {
      Frag B;
      W0 = ldA(-c0); B = ldB(0, c0); MF(a0[3], W1, B); MF(a0[2], W2, B); MF(a0[1], W3, B); MF(a0[0], W0, B); c0 += 32;
      W1 = ldA(-c0); B = ldB(0, c0); MF(a0[3], W2, B); MF(a0[2], W3, B); MF(a0[1], W0, B); MF(a0[0], W1, B); c0 += 32;
      W2 = ldA(-c0); B = ldB(0, c0); MF(a0[3], W3, B); MF(a0[2], W0, B); MF(a0[1], W1, B); MF(a0[0], W2, B); c0 += 32;
      W3 = ldA(-c0); B = ldB(0, c0); MF(a0[3], W0, B); MF(a0[2], W1, B); MF(a0[1], W2, B); MF(a0[0], W3, B); c0 += 32;
    }
#pragma unroll 1
    for (int it = 0; it < 4; ++it) {
      Frag B, C;
#define STEP2(Wn, Wa, Wb, Wc) \
      Wn = ldA(-c0); B = ldB(0, c0); C = ldB(1, c0); \
      MF(a0[3], Wa, B); MF(a1[3], Wa, C); MF(a0[2], Wb, B); MF(a1[2], Wb, C); \
      MF(a0[1], Wc, B); MF(a1[1], Wc, C); MF(a0[0], Wn, B); MF(a1[0], Wn, C); c0 += 32;
      STEP2(W0, W1, W2, W3) STEP2(W1, W2, W3, W0) STEP2(W2, W3, W0, W1) STEP2(W3, W0, W1, W2)
    }
    {
      Frag B = ldB(0, c0), C = ldB(1, c0);
      MF(a0[3], W1, B); MF(a1[3], W1, C);
    }
  }
  __syncthreads();
  LAS float* xch = (LAS float*)(cbase);
  if (wv == 1) {
#pragma unroll
    for (int t = 0; t < 4; ++t)
#pragma unroll
      for (int i = 0; i < 4; ++i) xch[(t * 4 + i) * 64 + lane] = a1[t][i];
  }
  __syncthreads();
  if (wv == 0) {
#pragma unroll
    for (int t = 0; t < 4; ++t)
#pragma unroll
      for (int i = 0; i < 4; ++i) a0[t][i] = a1[t][i] + xch[(t * 4 + i) * 64 + lane];
  }
  {
    const int T = wv == 0 ? 1 : 0;
    const float ki = kinv[d0 + ch];
    const float dk = bf2f(dskip[d0 + ch]);
    LAS u16* sbuf = (LAS u16*)(cbase + 4096);
    const int l1 = 8 * T + (r >> 1), sft = r & 1;
#pragma unroll
    for (int t = 0; t < 4; ++t)
#pragma unroll
      for (int i = 0; i < 4; ++i) {
        const int l = 128 * l1 + 32 * t + 8 * g + 2 * i + sft;
        const int c = l >> 3;
        const _Float16 uh = *(LAS _Float16*)(cbase + UB0 + 16 * (c + 2 * (c >> 4) + 4) + 2 * (l & 7));
        const float y = bfr(a0[t][i] * ki);
        const float z = bfr((float)uh * dk);
        const __bf16 sv = (__bf16)(y + z);
        sbuf[l] = __builtin_bit_cast(u16, sv);
      }
  }
  __syncthreads();
  {
    const __amdgpu_buffer_rsrc_t rs = __builtin_amdgcn_make_buffer_rsrc(sT, 0, D_ * L_ * 2, 0x00020000);
#pragma unroll
    for (int c2 = 0; c2 < 2; ++c2) {
      const u32x4 v = *(LAS u32x4*)(smem + c2 * CV_CH + 4096 + 16 * tid);
      __builtin_amdgcn_raw_buffer_store_b128(v, rs, (unsigned)(((d0 + c2) * L_ + 8 * tid) * 2), 0, 16);
    }
  }
  WAIT_VM(0);
  __syncthreads();
  if (tid == 0) __hip_atomic_fetch_add((GAS unsigned*)(cnt + (d0 >> 6) * 64), 1u, __ATOMIC_RELAXED, __HIP_MEMORY_SCOPE_AGENT);
}

__device__ __forceinline__ void gemm2_role(LAS char* smem, int gb, const u16* __restrict__ h, const u16* __restrict__ wd,
                                           const u16* __restrict__ x, const u16* sT, unsigned* cnt,
                                           u16* __restrict__ out) {
  const int tid = threadIdx.x, lane = tid & 63;
  const int w = __builtin_amdgcn_readfirstlane(tid >> 6);
  const int xcd = gb & 7, idx = gb >> 3;
  const int mt = (xcd & 3) * 8 + (idx & 7), nt = (xcd >> 2) * 4 + (idx >> 3);
  const int m0 = mt * 64, n0 = nt * 64;
  const int wm = w >> 1, wn = w & 1;
  const u16* asrc[2]; const u16* bsrc[2];
#pragma unroll
  for (int i = 0; i < 2; ++i) {
    const int row = 8 * (2 * w + i) + (lane >> 3);
    const int c = (lane & 7) ^ ((row >> 1) & 7);
    asrc[i] = h + (size_t)(m0 + row) * F_ + 8 * c;
    const int kb = 2 * w + i, cbk = lane >> 4;
    const int k = 8 * kb + (((lane & 15) >> 1) ^ (4 * (cbk & 1)));
    bsrc[i] = wd + (size_t)k * D_ + n0 + 16 * cbk + 8 * (lane & 1);
  }
  auto stage = [&](int s) {
    LAS char* base = smem + (s % G2_NS) * G2_STAGE;
#pragma unroll
    for (int i = 0; i < 2; ++i) glds16(asrc[i] + s * 64, base + (2 * w + i) * 1024);
#pragma unroll
    for (int i = 0; i < 2; ++i) glds16(bsrc[i] + (size_t)s * 64 * D_, base + 8192 + (2 * w + i) * 1024);
  };
  const int mrow = 32 * wm + (lane & 31), hh = lane >> 5;
  int xoff[4];
#pragma unroll
  for (int ks = 0; ks < 4; ++ks) xoff[ks] = mrow * 128 + (((2 * ks + hh) ^ ((mrow >> 1) & 7)) << 4);
  const int g4 = (lane >> 4) & 1, q = (lane & 15) >> 2, p = lane & 3;
  const int cbl = p & 1, grp = 2 * g4 + (p >> 1);
  const int woff0 = 8192 + (hh * 4 + 2 * wn + cbl) * 256 + (4 * cbl + q) * 32 + grp * 8;
  const int woff1 = 8192 + (hh * 4 + 2 * wn + cbl) * 256 + (4 * (1 - cbl) + q) * 32 + grp * 8;
  f32x16 acc = {};
  constexpr int NST = F_ / 64;
#pragma unroll
  for (int s = 0; s < G2_NS - 1; ++s) stage(s);
#pragma unroll 1
  for (int s = 0; s < NST; ++s) {
    if (s < NST - 3) WAIT_VM(12);
    else if (s == NST - 3) WAIT_VM(8);
    else if (s == NST - 2) WAIT_VM(4);
    else WAIT_VM(0);
    BARRIER();
    if (s + G2_NS - 1 < NST) stage(s + G2_NS - 1);
    LAS char* base = smem + (s % G2_NS) * G2_STAGE;
#pragma unroll
    for (int ks = 0; ks < 4; ++ks) {
      Frag xf; xf.u = *(LAS u32x4*)(base + xoff[ks]);
      Frag wf;
      wf.s.a = trread(base + woff0 + ks * 2048);
      wf.s.b = trread(base + woff1 + ks * 2048);
      acc = __builtin_amdgcn_mfma_f32_32x32x16_bf16(wf.bf, xf.bf, acc, 0, 0, 0);
    }
  }
  if (w == 0) {
    unsigned spins = 0;
    while ((unsigned)__builtin_amdgcn_readfirstlane(
               __hip_atomic_load((GAS unsigned*)(cnt + nt * 64), __ATOMIC_RELAXED, __HIP_MEMORY_SCOPE_AGENT)) < 32u) {
      __builtin_amdgcn_s_sleep(2);
      if (++spins > (1u << 22)) break;
    }
  }
  __syncthreads();
  {
    const __amdgpu_buffer_rsrc_t rs = __builtin_amdgcn_make_buffer_rsrc((void*)sT, 0, D_ * L_ * 2, 0x00020000);
#pragma unroll
    for (int i = 0; i < 2; ++i) {
      const int c = tid + 256 * i, dr = c >> 3, cc = c & 7;
      const u32x4 v = __builtin_amdgcn_raw_buffer_load_b128(rs, (unsigned)(((n0 + dr) * L_ + m0 + 8 * cc) * 2), 0, 16);
      *(LAS u32x4*)(smem + dr * 144 + cc * 16) = v;
    }
  }
  const size_t go = (size_t)(m0 + mrow) * D_ + n0 + 32 * wn + 16 * hh;
  const u32x4 xa = *(const u32x4*)(x + go), xb = *(const u32x4*)(x + go + 8);
  __syncthreads();
  float o[16];
#pragma unroll
  for (int a = 0; a < 4; ++a) {
    const s16x4 sv = trread(smem + (32 * wn + 16 * hh + 4 * a + q) * 144 + (32 * wm + 16 * g4 + 4 * p) * 2);
#pragma unroll
    for (int i = 0; i < 4; ++i) {
      const int e = 4 * a + i;
      const float ov = bfr(acc[e]);
      const float gv = bfr(ov * __builtin_amdgcn_rcpf(1.0f + __builtin_amdgcn_exp2f(-1.4426950408889634f * ov)));
      const float sf = bf2f((unsigned)(u16)sv[i]);
      const unsigned xw = e < 8 ? xa[e >> 1] : xb[(e - 8) >> 1];
      const float xf = (e & 1) ? bfhi(xw) : bflo(xw);
      o[e] = xf + bfr(sf * gv);
    }
  }
  u32x4 va, vb;
#pragma unroll
  for (int j = 0; j < 4; ++j) { va[j] = pkbf(o[2 * j], o[2 * j + 1]); vb[j] = pkbf(o[8 + 2 * j], o[9 + 2 * j]); }
  *(u32x4*)(out + go) = va;
  *(u32x4*)(out + go + 8) = vb;
}

__global__ __launch_bounds__(256, 2) void k2_down_conv(const u16* __restrict__ h, const u16* __restrict__ wd,
                                                       const u16* __restrict__ x, const u16* __restrict__ uT,
                                                       const u16* __restrict__ Kr, const float* __restrict__ kinv,
                                                       const u16* __restrict__ dskip, u16* sT, unsigned* cnt,
                                                       u16* __restrict__ out) {
  extern __shared__ __attribute__((aligned(16))) char smem_[];
  LAS char* smem = (LAS char*)smem_;
  const int b = blockIdx.x;
  const int role = ((b >> 3) & 1) ^ (b >> 8);
  const int id = ((b >> 4) << 3) | (b & 7);
  if (role == 0) conv_role(smem, id, uT, Kr, kinv, dskip, sT, cnt);
  else gemm2_role(smem, id, h, wd, x, sT, cnt, out);
}

extern "C" void kernel_launch(void* const* d_in, const int* in_sizes, int n_in, void* d_out, int out_size,
                              void* d_ws, size_t ws_size, hipStream_t stream) {
  const u16* x = (const u16*)d_in[0];
  const u16* lAr = (const u16*)d_in[1];
  const u16* Aim = (const u16*)d_in[2];
  const u16* Cr = (const u16*)d_in[3];
  const u16* Ci = (const u16*)d_in[4];
  const u16* ldt = (const u16*)d_in[5];
  const u16* dsk = (const u16*)d_in[6];
  const u16* wg = (const u16*)d_in[8];
  const u16* wu = (const u16*)d_in[9];
  const u16* wd = (const u16*)d_in[10];
  char* ws = (char*)d_ws;
  u16* xn = (u16*)(ws + OFF_XN);
  u16* uT = (u16*)(ws + OFF_UT);
  u16* Kr = (u16*)(ws + OFF_KR);
  float* kinv = (float*)(ws + OFF_KINV);
  unsigned* cnt = (unsigned*)(ws + OFF_CNT);
  u16* h = (u16*)(ws + OFF_H);
  u16* sT = (u16*)(ws + OFF_ST);
  (void)hipFuncSetAttribute((const void*)k1_gemm1, hipFuncAttributeMaxDynamicSharedMemorySize, G1_LDS);
  (void)hipFuncSetAttribute((const void*)k2_down_conv, hipFuncAttributeMaxDynamicSharedMemorySize, K2_LDS);
  k0_prep<<<K0_LN_BLOCKS + D_, 256, 0, stream>>>(x, lAr, Aim, Cr, Ci, ldt, xn, uT, Kr, kinv, cnt);
  k1_gemm1<<<256, 256, G1_LDS, stream>>>(xn, wg, wu, h);
  k2_down_conv<<<512, 256, K2_LDS, stream>>>(h, wd, x, uT, Kr, kinv, dsk, sT, cnt, (u16*)d_out);
}
```

```cpp
#include <hip/hip_runtime.h>
#include <hip/hip_bf16.h>
#include <stdint.h>

#if defined(__HIP_DEVICE_COMPILE__)
#pragma clang attribute push(__attribute__((target("no-packed-fp32-ops"))), apply_to = function)
#endif

#define U2F(x) __builtin_bit_cast(float, (unsigned)(x))
#define F2U(x) __builtin_bit_cast(unsigned, (float)(x))
#define TIDX ((int)__builtin_amdgcn_workitem_id_x())
#define BIDX ((int)__builtin_amdgcn_workgroup_id_x())
#define SYNCTHREADS() do { __builtin_amdgcn_fence(__ATOMIC_RELEASE, "workgroup"); __builtin_amdgcn_s_barrier(); __builtin_amdgcn_fence(__ATOMIC_ACQUIRE, "workgroup"); } while (0)

typedef unsigned short u16;
typedef __attribute__((ext_vector_type(4))) short s16x4;
typedef __attribute__((ext_vector_type(8))) __bf16 bf16x8;
typedef __attribute__((ext_vector_type(8))) _Float16 f16x8;
typedef __attribute__((ext_vector_type(2))) _Float16 f16x2;
typedef __attribute__((ext_vector_type(2))) __fp16 h16x2;
typedef __attribute__((ext_vector_type(4))) float f32x4;
typedef __attribute__((ext_vector_type(16))) float f32x16;
typedef __attribute__((ext_vector_type(4))) unsigned u32x4;
typedef __attribute__((ext_vector_type(2))) unsigned u32x2;
#define LAS __attribute__((address_space(3)))
#define GAS __attribute__((address_space(1)))

constexpr int L_ = 2048, D_ = 512, N_ = 128, F_ = 1792;
constexpr int KR_ = 2240;
constexpr int KZ_ = 2080;

constexpr size_t OFF_XN = 0;
constexpr size_t OFF_UT = 2u << 20;
constexpr size_t OFF_KR = 4u << 20;
constexpr size_t OFF_KINV = 7u << 20;
constexpr size_t OFF_CNT = (7u << 20) + 65536;
constexpr size_t OFF_H = 8u << 20;
constexpr size_t OFF_ST = 16u << 20;

__device__ __forceinline__ float bf2f(unsigned v) { return U2F(v << 16); }
__device__ __forceinline__ float bflo(unsigned v) { return U2F(v << 16); }
__device__ __forceinline__ float bfhi(unsigned v) { return U2F(v & 0xffff0000u); }
__device__ __forceinline__ float bfr(float f) { return (float)(__bf16)f; }
__device__ __forceinline__ unsigned pkbf(float a, float b) {
  typedef __attribute__((ext_vector_type(2))) __bf16 bf16x2;
  bf16x2 v; v.x = (__bf16)a; v.y = (__bf16)b;
  return __builtin_bit_cast(unsigned, v);
}
__device__ __forceinline__ unsigned pkh(float a, float b) {
  f16x2 v; v.x = (_Float16)a; v.y = (_Float16)b;
  return __builtin_bit_cast(unsigned, v);
}
__device__ __forceinline__ void glds16(const void* g, LAS void* l) {
  __builtin_amdgcn_global_load_lds((const GAS void*)g, l, 16, 0, 0);
}
__device__ __forceinline__ void glds16_sc1(const void* g, LAS void* l) {
  __builtin_amdgcn_global_load_lds((const GAS void*)g, l, 16, 0, 16);
}
__device__ __forceinline__ s16x4 trread(LAS char* p) {
  return __builtin_amdgcn_ds_read_tr16_b64_v4i16((LAS s16x4*)p);
}
union Frag { struct { s16x4 a, b; } s; bf16x8 bf; f16x8 h; u32x4 u; };

#define WAIT_VM(n) asm volatile("s_waitcnt vmcnt(" #n ")" ::: "memory")
#define WAIT_VMN(n) asm volatile("s_waitcnt vmcnt(%0)" :: "i"(n) : "memory")
#define BARRIER() do { asm volatile("s_waitcnt lgkmcnt(0)" ::: "memory"); __builtin_amdgcn_s_barrier(); asm volatile("" ::: "memory"); } while (0)

#define DS_TR(dst, addr, off) asm volatile("ds_read_b64_tr_b16 %0, %1 offset:%2" : "=v"(dst) : "v"(addr), "i"(off))
#define DS_B128(dst, addr, off) asm volatile("ds_read_b128 %0, %1 offset:%2" : "=v"(dst) : "v"(addr), "i"(off))
#define WAIT_LGKM(n) asm volatile("s_waitcnt lgkmcnt(" #n ")" ::: "memory")
#define SCHED() __builtin_amdgcn_sched_barrier(0)
#define MFMA32_BF16(acc, a, b) asm volatile("v_mfma_f32_32x32x16_bf16 %0, %1, %2, %0" : "+v"(acc) : "v"(a), "v"(b))
#define MFMA_DRAIN() asm volatile("s_nop 15\n\ts_nop 7" ::: "memory")
#define STAMPAT(i) do { } while (0)
#define STAMPRT(k, i) do { } while (0)
#define RT2(i) do { } while (0)

__device__ __forceinline__ float wave_sum(float v) {
  v += __builtin_bit_cast(float, __builtin_amdgcn_update_dpp(0, __builtin_bit_cast(int, v), 0xB1, 0xf, 0xf, true));
  v += __builtin_bit_cast(float, __builtin_amdgcn_update_dpp(0, __builtin_bit_cast(int, v), 0x4E, 0xf, 0xf, true));
  v += __builtin_bit_cast(float, __builtin_amdgcn_update_dpp(0, __builtin_bit_cast(int, v), 0x141, 0xf, 0xf, true));
  v += __builtin_bit_cast(float, __builtin_amdgcn_update_dpp(0, __builtin_bit_cast(int, v), 0x140, 0xf, 0xf, true));
  const float r0 = __builtin_bit_cast(float, __builtin_amdgcn_readlane(__builtin_bit_cast(int, v), 0));
  const float r1 = __builtin_bit_cast(float, __builtin_amdgcn_readlane(__builtin_bit_cast(int, v), 16));
  const float r2 = __builtin_bit_cast(float, __builtin_amdgcn_readlane(__builtin_bit_cast(int, v), 32));
  const float r3 = __builtin_bit_cast(float, __builtin_amdgcn_readlane(__builtin_bit_cast(int, v), 48));
  return (r0 + r1) + (r2 + r3);
}
__device__ __forceinline__ void cexp_p(float arl2, float wh, float wl, float p, float& re, float& im) {
  const float mag = __builtin_amdgcn_exp2f(arl2 * p);
  const float fr = __builtin_fmaf(wl, p, __builtin_amdgcn_fractf(wh * p));
  re = mag * __builtin_amdgcn_cosf(fr);
  im = mag * __builtin_amdgcn_sinf(fr);
}
__device__ __forceinline__ void split_h(float a, float b, unsigned& hi, unsigned& lo) {
  h16x2 h = __builtin_amdgcn_cvt_pkrtz(a, b);
  float ra = a - (float)h.x, rb = b - (float)h.y;
  h16x2 l = __builtin_amdgcn_cvt_pkrtz(ra, rb);
  hi = __builtin_bit_cast(unsigned, h);
  lo = __builtin_bit_cast(unsigned, l);
}

__global__ __launch_bounds__(512) void k0_ln(const u16* __restrict__ x, u16* __restrict__ xn, u16* __restrict__ uT,
                                             unsigned* __restrict__ cnt, const u16* __restrict__ lAr,
                                             const u16* __restrict__ Aim, const u16* __restrict__ Cr,
                                             const u16* __restrict__ Ci, const u16* __restrict__ ldt,
                                             float* __restrict__ prm, const u16* __restrict__ wg,
                                             const u16* __restrict__ wu) {
  __shared__ __attribute__((aligned(16))) unsigned tile[8 * 260];
  const int tid = TIDX, w = tid >> 6, lane = tid & 63;
  const int lb = BIDX;
  STAMPRT(0, 0);
  if (lb == 0 && tid < 64) cnt[tid * 64] = 0u;
  const int row = lb * 8 + w;
  const u32x4 v = *(const u32x4*)(x + (size_t)row * D_ + lane * 8);
  unsigned warm = 0;
  if (w < 4) {
    const int d = 2 * lb + (w >> 1), n = d * N_ + (w & 1) * 64 + lane;
    const unsigned vdt = ldt[d];
    const unsigned a = *(const unsigned*)(Cr + d * N_ + 2 * lane), bb = *(const unsigned*)(Ci + d * N_ + 2 * lane);
    const unsigned v0 = lAr[n], v1 = Aim[n], v2 = Cr[n], v3 = Ci[n];
    const float dt = bfr(__builtin_expf(bf2f(vdt)));
    const float sb = wave_sum((__builtin_fabsf(bflo(a)) + __builtin_fabsf(bfhi(a))) + (__builtin_fabsf(bflo(bb)) + __builtin_fabsf(bfhi(bb)))) * dt * 2.0f;
    const int ex = (int)((F2U(sb) >> 23) & 0xff) - 127;
    int se = 14 - ex;
    se = se > 100 ? 100 : (se < -100 ? -100 : se);
    const float cs = dt * 2.0f * U2F((unsigned)(127 + se) << 23);
    const float ar = -bfr(__builtin_expf(bf2f(v0))) * dt;
    const float ai = bf2f(v1) * dt;
    const float w0 = ai * 0.15915494309189535f;
    const float wh = U2F(F2U(w0) & 0xfffff000u);
    const float wl = __builtin_fmaf(ai, 6.4206383e-09f, __builtin_fmaf(ai, 0.15915494309189535f, -wh));
    float* pd = prm + (size_t)d * 704 + (w & 1) * 64 + lane;
    pd[0] = ar * 1.4426950408889634f;
    pd[128] = wh;
    pd[256] = wl;
    pd[384] = bf2f(v2) * cs;
    pd[512] = bf2f(v3) * cs;
    if ((w & 1) == 0 && lane == 0) pd[640] = U2F((unsigned)(127 - se) << 23);
  }
  float f[8];
#pragma unroll
  for (int j = 0; j < 4; ++j) { f[2 * j] = bflo(v[j]); f[2 * j + 1] = bfhi(v[j]); }
  const float mean = wave_sum(((f[0] + f[1]) + (f[2] + f[3])) + ((f[4] + f[5]) + (f[6] + f[7]))) * (1.0f / D_);
  float a = 0.f;
#pragma unroll
  for (int j = 0; j < 8; ++j) { const float d = f[j] - mean; a = __builtin_fmaf(d, d, a); }
  const float var = wave_sum(a) * (1.0f / D_);
  const float mb = bfr(mean);
  const float sd = bfr(__builtin_sqrtf(bfr(bfr(var) + 1e-5f)));
  const float rs = __builtin_amdgcn_rcpf(sd);
  u32x4 pb; unsigned ph[4];
#pragma unroll
  for (int j = 0; j < 4; ++j) {
    const unsigned t = pkbf(f[2 * j] - mb, f[2 * j + 1] - mb);
    const unsigned o = pkbf(bflo(t) * rs, bfhi(t) * rs);
    pb[j] = o;
    ph[j] = __builtin_bit_cast(unsigned, __builtin_amdgcn_cvt_pkrtz(bflo(o), bfhi(o)));
  }
  *(u32x4*)(xn + (size_t)row * D_ + lane * 8) = pb;
  *(u32x4*)(tile + w * 260 + lane * 4) = (u32x4){ph[0], ph[1], ph[2], ph[3]};
  SYNCTHREADS();
  const u16* th = (const u16*)tile;
  unsigned c[8];
#pragma unroll
  for (int r = 0; r < 8; ++r) c[r] = th[r * 520 + tid];
  u32x4 o;
#pragma unroll
  for (int j = 0; j < 4; ++j) o[j] = c[2 * j] | (c[2 * j + 1] << 16);
  *(u32x4*)(uT + (size_t)tid * L_ + lb * 8) = o;
  asm volatile("s_waitcnt vmcnt(0)" : "+v"(warm) :: "memory");
  STAMPRT(0, 1);
}

constexpr int G1_A = 16384, G1_W = 32768, G1_STAGE = G1_A + G1_W;
constexpr int KF_LDS = 163840;
#define HSWAP 0
constexpr int HS_PART = 147456;
constexpr int HS_PRM = HS_PART + 4 * 2816;
constexpr int CV_KS = 4672, CV_US = 4736, CV_CH = 4 * CV_KS + 2 * CV_US;
constexpr int KB2 = 64, KB4 = 64 + CV_KS, KB6 = 64 + 2 * CV_KS, KB0 = 64 + 3 * CV_KS - 16;
constexpr int UB0 = 4 * CV_KS + 64, UB1 = UB0 + CV_US;
constexpr int CV_XCH = 57344;
constexpr int CV_KL = 73728;
constexpr int G2_BR = 81920, G2_NB = 10, G2_NA = 8;
constexpr int ST_OFF = 5 * 8192;
constexpr int CNT_H = 0, CNT_S = 16;

__device__ __forceinline__ float gelu_t(float x) {
  const float c1 = -2.0f * 0.7978845608028654f * 1.4426950408889634f;
  const float c2 = c1 * 0.044715f;
  float a = x * __builtin_fmaf(x * x, c2, c1);
  float e = __builtin_amdgcn_exp2f(a);
  return x * __builtin_amdgcn_rcpf(1.0f + e);
}
__device__ __forceinline__ void poll_ge(unsigned* p, unsigned want) {
  unsigned spins = 0;
  while ((unsigned)__builtin_amdgcn_readfirstlane(__hip_atomic_load((GAS unsigned*)p, __ATOMIC_RELAXED, __HIP_MEMORY_SCOPE_AGENT)) < want) {
    __builtin_amdgcn_s_sleep(1);
    if (++spins > (1u << 22)) break;
  }
}

struct KIdx { u16 v[2304]; };
constexpr KIdx make_kidx() {
  KIdx t{};
  for (int x = 0; x < 2304; ++x) {
    const int idx = KZ_ - x;
    int o = 21 * 32 + 1;
    if (idx >= 0 && idx < L_) {
      int p = idx;
      int sh = idx >= 1024 ? 3 : (idx >= 512 ? 2 : (idx >= 256 ? 1 : 0));
      if (sh) {
        const int q = idx >> sh, rem = idx & ((1 << sh) - 1), half = 1 << (sh - 1);
        p = (q + ((rem > half || (rem == half && (q & 1))) ? 1 : 0)) << sh;
      }
      const int r = p & 7, cc = p >> 3, c1 = cc >> 5, c0 = cc & 31;
      const int rw = c1 == 0 ? r : (c1 == 1 ? 8 + (r >> 1) : (c1 < 4 ? 8 + 2 * c1 + (r >> 2) : 12 + c1));
      o = rw * 32 + c0;
    }
    t.v[x] = (u16)(o * 4);
  }
  return t;
}
__device__ const KIdx g_kidx = make_kidx();

struct G1Frags { Frag x[2]; Frag w[2]; };
struct KFArgs {
  const u16 *x, *dsk, *wg, *wu, *wd;
  const u16 *xn, *uT; const float* prm; u16 *h, *sT; unsigned* cnt; u16* out;
};

__device__ __forceinline__ unsigned mov32(unsigned x) { unsigned r; asm("v_mov_b32 %0, %1" : "=v"(r) : "v"(x)); return r; }
__device__ __forceinline__ void split_h2(float a, float b, unsigned& hi, unsigned& lo) {
  const h16x2 h = __builtin_amdgcn_cvt_pkrtz(a, b);
  f16x2 l;
  l.x = (_Float16)__builtin_fmaf((float)h.x, -1.0f, a);
  l.y = (_Float16)__builtin_fmaf((float)h.y, -1.0f, b);
  hi = __builtin_bit_cast(unsigned, h);
  lo = __builtin_bit_cast(unsigned, l);
}
#define HBAR() do { SCHED(); __builtin_amdgcn_s_barrier(); SCHED(); } while (0)
__device__ __forceinline__ void kf_helper(LAS char* smem, const KFArgs& A, int j, int lane, int g2m0, int g2n0, unsigned* hwait, unsigned* swait) {
  const int b = BIDX, ch = j >> 2, nq = j & 3, d = 2 * b + ch;
  LAS float* prm = (LAS float*)(smem + HS_PRM) + j * 160;
  LAS float* part = (LAS float*)(smem + HS_PART) + (j >> 1) * 704;
  const float* pg = A.prm + (size_t)d * 704 + nq * 32 + (lane & 31);
  const float p0 = pg[0], p1 = pg[128], p2 = pg[256], p3 = pg[384], p4 = pg[512];
  const float kinv = A.prm[(size_t)d * 704 + 640];
  const int uc = 64 * nq + lane;
  const u16* ur = A.uT + (size_t)d * L_ + 8 * uc;
  const u32x4 ua = *(const u32x4*)ur;
  const unsigned unx = ur[uc < 255 ? 8 : 0];
  const u32x4 kx0 = *(const u32x4*)(g_kidx.v + 8 * (4 + uc));
  const int c2 = lane < 4 ? lane : 256 + (lane < 21 ? lane : 20);
  const u32x4 kx1 = *(const u32x4*)(g_kidx.v + 8 * c2);
  asm volatile("" ::: "memory");
  __builtin_amdgcn_s_barrier();
  asm volatile("" ::: "memory");
  if (lane < 32) { prm[lane] = p0; prm[32 + lane] = p1; prm[64 + lane] = p2; prm[96 + lane] = p3; prm[128 + lane] = p4; }
  if ((j & 1) == 0 && lane == 0) part[21 * 32 + 1] = 0.f;
  const int row = lane & 31, hh = lane >> 5;
  int prow;
  if (row < 8) prow = row;
  else if (row < 12) prow = 256 + 2 * (row - 8);
  else if (row < 16) prow = 512 + 256 * ((row - 12) >> 1) + 4 * ((row - 12) & 1);
  else prow = 1024 + 256 * (row - 16);
  prow = row < 21 ? prow : 0;
  const float pA = (float)prow, pB = (float)(8 * row);
  f32x16 acc = {};
#pragma unroll
  for (int ks = 0; ks < 4; ++ks) {
    const int n0 = ks * 8 + hh * 4;
    const f32x4 va = *(LAS f32x4*)(prm + n0), vh = *(LAS f32x4*)(prm + 32 + n0), vl = *(LAS f32x4*)(prm + 64 + n0);
    const f32x4 vr = *(LAS f32x4*)(prm + 96 + n0), vi = *(LAS f32x4*)(prm + 128 + n0);
    Frag Ah, Al, Bh, Bl;
#pragma unroll
    for (int q = 0; q < 4; ++q) {
      float er, ei, yr, yi;
      cexp_p(va[q], vh[q], vl[q], pA, er, ei);
      cexp_p(va[q], vh[q], vl[q], pB, yr, yi);
      const float xr = vr[q] * er - vi[q] * ei, xi = vr[q] * ei + vi[q] * er;
      unsigned h0, l0, h1, l1;
      split_h(xr, xi, h0, l0);
      split_h(yr, -yi, h1, l1);
      Ah.u[q] = h0; Al.u[q] = l0; Bh.u[q] = h1; Bl.u[q] = l1;
      const int qi = 4 * ks + q + 1;
      if (qi == 2 || qi == 4 || qi == 6 || qi == 8 || qi == 10 || qi == 12 || qi == 14) {
        asm volatile("" : "+v"(Ah.u), "+v"(Al.u), "+v"(Bh.u), "+v"(Bl.u));
        HBAR();
      }
    }
    acc = __builtin_amdgcn_mfma_f32_32x32x16_f16(Al.h, Bh.h, acc, 0, 0, 0);
    acc = __builtin_amdgcn_mfma_f32_32x32x16_f16(Ah.h, Bl.h, acc, 0, 0, 0);
    acc = __builtin_amdgcn_mfma_f32_32x32x16_f16(Ah.h, Bh.h, acc, 0, 0, 0);
  }
  STAMPAT(3);
  if ((j & 1) == 0) {
#pragma unroll
    for (int i = 0; i < 12; ++i) {
      const int rr = (i & 3) + 8 * (i >> 2) + 4 * hh;
      if (rr < 21) part[rr * 32 + row] = acc[i];
    }
  }
  BARRIER();
  if (j & 1) {
#pragma unroll
    for (int i = 0; i < 12; ++i) {
      const int rr = (i & 3) + 8 * (i >> 2) + 4 * hh;
      if (rr < 21) part[rr * 32 + row] += acc[i];
    }
  }
  STAMPAT(4);
  BARRIER();
  STAMPAT(5);
  unsigned warm = 0;
  {
    const LAS char* t0 = smem + HS_PART + (2 * ch) * 2816;
    LAS char* cbase = smem + ch * CV_CH;
    auto expand = [&](const u32x4& kx, int c) {
      float v[8];
#pragma unroll
      for (int e = 0; e < 8; ++e) {
        const unsigned o = (e & 1) ? (kx[e >> 1] >> 16) : (kx[e >> 1] & 0xffffu);
        v[e] = *(const LAS float*)(t0 + o) + *(const LAS float*)(t0 + o + 2816);
      }
      u32x4 o;
#pragma unroll
      for (int e = 0; e < 4; ++e) o[e] = pkh(v[2 * e], v[2 * e + 1]);
      *(LAS u32x4*)(cbase + KB0 + 16 * c) = o;
      LAS unsigned* q2 = (LAS unsigned*)(cbase + KB2 + 16 * c - 4);
      LAS u32x2* q4 = (LAS u32x2*)(cbase + KB4 + 16 * c - 8);
      LAS unsigned* q6 = (LAS unsigned*)(cbase + KB6 + 16 * c - 12);
#pragma unroll
      for (int e = 0; e < 4; ++e) { q2[e] = o[e]; q6[e] = o[e]; }
      q4[0] = (u32x2){o[0], o[1]}; q4[1] = (u32x2){o[2], o[3]};
    };
    expand(kx0, 4 + uc);
    if (nq == 0) {
      if (lane < 21) expand(kx1, c2);
      if (lane == 0) *(LAS float*)(cbase) = kinv;
    }
    const unsigned nx = (uc < 255) ? unx : 0u;
    const int P = uc + 2 * (uc >> 4) + 4;
    *(LAS u32x4*)(cbase + UB0 + 16 * P) = ua;
    u32x4 s1;
    s1[0] = (ua[0] >> 16) | (ua[1] << 16); s1[1] = (ua[1] >> 16) | (ua[2] << 16);
    s1[2] = (ua[2] >> 16) | (ua[3] << 16); s1[3] = (ua[3] >> 16) | (nx << 16);
    *(LAS u32x4*)(cbase + UB1 + 16 * P) = s1;
    if (uc == 0) {
      const u32x4 z = {0u, 0u, 0u, 0u};
      *(LAS u32x4*)(cbase + UB0) = z; *(LAS u32x4*)(cbase + UB0 + 16) = z;
      *(LAS u32x4*)(cbase + UB1) = z; *(LAS u32x4*)(cbase + UB1 + 16) = (u32x4){0u, 0u, 0u, ua[0] << 16};
    }
  }
  STAMPAT(6);
  BARRIER();
  STAMPAT(7);
  {
    constexpr int NST = F_ / 64;
    const char* bbase = (const char*)(A.wd + g2n0);
    const int r = 8 * j + (lane >> 3);
    const unsigned boff = (unsigned)(r * D_) * 2u + (unsigned)((((lane & 7) >> 1) ^ (r & 2)) * 32 + (lane & 1) * 16);
#define G2_LDB(s) glds16(bbase + (long)(s) * (128 * D_) + boff, smem + G2_BR + ((s) % G2_NB) * 8192 + j * 1024)
#pragma unroll
    for (int s = 0; s < G2_NB; ++s) G2_LDB(s);
    BARRIER();
    if (j == 0) poll_ge(hwait, 112u);
    BARRIER();
    asm volatile("s_waitcnt vmcnt(9)" : "+v"(warm) :: "memory");
    BARRIER();
    unsigned sflag = 0;
    u32x4 sv;
    const int sc = j * 64 + lane;
#pragma unroll
    for (int s = 0; s < NST - 1; ++s) {
      int n = 0;
      if (s + 1 < G2_NB) n = (G2_NB - 1 - (s + 1)) + s;
      else { for (int jj = s + 2 - G2_NB; jj < s; ++jj) n += (jj + G2_NB < NST ? 1 : 0); }
      if (s > 18) n += 1;
      if (s < 22) WAIT_VMN(n);
      if (s == 22) asm volatile("s_waitcnt vmcnt(0)" : "+v"(sflag) :: "memory");
      BARRIER();
      if (s + G2_NB < NST) G2_LDB(s + G2_NB);
      if (s == 18) asm volatile("global_load_dword %0, %1, off sc1" : "=&v"(sflag) : "v"(swait) : "memory");
      if (s == 22) {
        if ((unsigned)__builtin_amdgcn_readfirstlane(sflag) < 256u) poll_ge(swait, 256u);
        const __amdgpu_buffer_rsrc_t rs = __builtin_amdgcn_make_buffer_rsrc(A.sT, 0, D_ * L_ * 2, 0x00020000);
        const unsigned so = (unsigned)(((g2n0 + (sc >> 3)) * L_ + g2m0 + 8 * (sc & 7)) * 2);
        asm volatile("buffer_load_dwordx4 %0, %1, %2, 0 offen sc1" : "=&v"(sv) : "v"(so), "s"(rs) : "memory");
      }
    }
    asm volatile("s_waitcnt vmcnt(0)" : "+v"(sv) :: "memory");
    *(LAS u32x4*)(smem + ST_OFF + (sc >> 3) * 144 + (sc & 7) * 16) = sv;
    BARRIER();
  }
}

__global__ __launch_bounds__(1024, 4) void kf_main(KFArgs A) {
  extern __shared__ __attribute__((aligned(16))) char smem_[];
  LAS char* smem = (LAS char*)smem_;
  const unsigned sbase = (unsigned)(size_t)smem;
  const int tid = TIDX, lane = tid & 63;
  const int w = __builtin_amdgcn_readfirstlane(tid >> 6);
  const int b = BIDX, xcd = b & 7, idx = b >> 3;
  const bool has_tile = idx < 28;
  const int g2mt = xcd * 4 + (idx & 3), g2nt = idx >> 2;
  const int g2m0 = g2mt * 64, g2n0 = g2nt * 64;
  STAMPAT(0); STAMPRT(1, 0); RT2(0);
  if (w >= 8) { kf_helper(smem, A, w - 8, lane, g2m0, g2n0, A.cnt + (CNT_H + (g2mt >> 1)) * 64, A.cnt + (CNT_S + g2nt) * 64); return; }

  unsigned* hcnt = nullptr;
  if (has_tile) {
    const int mt = (xcd & 3) * 4 + (idx & 3), nt = (xcd >> 2) * 7 + (idx >> 2);
    const int m0 = mt * 128, n0 = nt * 128;
    const char* abase = (const char*)(A.xn + (size_t)m0 * D_);
    const char* gbase = (const char*)(A.wg + n0);
    const char* ubase = (const char*)(A.wu + n0);
    const int arow = 16 * w + (lane >> 3);
    const unsigned aoff0 = (unsigned)(arow * D_ + 8 * ((lane & 7) ^ ((arow >> 1) & 7))) * 2u;
    const unsigned aoff1 = (unsigned)((arow + 8) * D_ + 8 * ((lane & 7) ^ (((arow + 8) >> 1) & 7))) * 2u;
    const int wk = 8 * w + (lane >> 3);
    const int fk = (wk >> 1) & 1;
    const unsigned woffg = (unsigned)(wk * F_) * 2u + (unsigned)((((lane & 7) >> 1) ^ fk) * 32 + (lane & 1) * 16);
    const unsigned woffu = (unsigned)(wk * F_) * 2u + (unsigned)((((lane & 7) >> 1) ^ (fk | 2)) * 32 + (lane & 1) * 16);
#define G1_LD(i, s, sb) do { \
    if ((i) == 0) glds16(abase + (s) * 128 + aoff0, smem + (sb) + (2 * w) * 1024); \
    if ((i) == 1) glds16(gbase + (long)(s) * (128 * F_) + woffg, smem + (sb) + G1_A + w * 1024); \
    if ((i) == 2) glds16(gbase + (long)(s) * (128 * F_) + 128 + woffg, smem + (sb) + G1_A + 8192 + w * 1024); \
    if ((i) == 3) glds16(abase + (s) * 128 + aoff1, smem + (sb) + (2 * w + 1) * 1024); \
    if ((i) == 4) glds16(ubase + (long)(s) * (128 * F_) + woffu, smem + (sb) + G1_A + 16384 + w * 1024); \
    if ((i) == 5) glds16(ubase + (long)(s) * (128 * F_) + 128 + woffu, smem + (sb) + G1_A + 24576 + w * 1024); } while (0)
#define G1_STAGE_LOAD(s, sb) do { G1_LD(0, s, sb); G1_LD(1, s, sb); G1_LD(2, s, sb); G1_LD(3, s, sb); G1_LD(4, s, sb); G1_LD(5, s, sb); } while (0)
    G1_STAGE_LOAD(0, 0);
    __builtin_amdgcn_s_barrier();
    G1_STAGE_LOAD(1, G1_STAGE);
    const int wm = w & 1, wn = w >> 1;
    const int mrow = 64 * wm + (lane & 31), hh = lane >> 5;
    unsigned xoff[4];
#pragma unroll
    for (int ks = 0; ks < 4; ++ks) xoff[ks] = sbase + mrow * 128 + (((2 * ks + hh) ^ ((mrow >> 1) & 7)) << 4);
    const int mat = (lane >> 4) & 1, q = (lane & 15) >> 2, p = lane & 3;
    const int pp = ((p & 1) << 1) | (p >> 1);
    const int fq = ((q >> 1) & 1) | (mat << 1);
    const unsigned wrow = sbase + G1_A + (mat * 2 + (wn >> 1)) * 8192 + (8 * hh + q) * 128 + pp * 8;
    const unsigned woff0 = wrow + (((2 * (wn & 1)) ^ fq) * 32);
    const unsigned woff1 = wrow + (((2 * (wn & 1) + 1) ^ fq) * 32);
    f32x16 acc[2][2];
#pragma unroll
    for (int t = 0; t < 2; ++t) { acc[t][0] = (f32x16){}; acc[t][1] = (f32x16){}; }
    G1Frags FA, FB;
#define RD_W(S, t, b0, b1, ks) do { DS_TR(S.w[t].s.a, (t) ? b1 : b0, (ks) * 2048); DS_TR(S.w[t].s.b, (t) ? b1 : b0, (ks) * 2048 + 512); } while (0)
#define RD_X(S, XA) do { DS_B128(S.x[0].u, XA, 0); DS_B128(S.x[1].u, XA, 4096); } while (0)
#define MM1(S, t, m) MFMA32_BF16(acc[t][m], S.w[t].u, S.x[m].u)
    WAIT_VM(6);
    BARRIER();
    STAMPAT(1);
    RD_X(FA, xoff[0]); RD_W(FA, 0, woff0, woff1, 0); RD_W(FA, 1, woff0, woff1, 0);
    SCHED();
    unsigned so = 0, sn = G1_STAGE, sp = 2 * G1_STAGE;
#define KSTEP(CUR, NXT, XA, KSN, L0, MORE) \
    WAIT_LGKM(2); SCHED(); MM1(CUR, 0, 0); RD_X(NXT, XA); SCHED(); \
    MM1(CUR, 0, 1); RD_W(NXT, 0, w0, w1, KSN); if (MORE) G1_LD(L0, s + 2, sp); SCHED(); \
    WAIT_LGKM(4); SCHED(); MM1(CUR, 1, 0); RD_W(NXT, 1, w0, w1, KSN); SCHED(); \
    MM1(CUR, 1, 1); if (MORE) G1_LD(L0 + 1, s + 2, sp); SCHED();
#define G1_BODY(MORE) { \
    const unsigned w0 = woff0 + so, w1 = woff1 + so; \
    const unsigned x1 = xoff[1] + so, x2 = xoff[2] + so, x3 = xoff[3] + so; \
    const unsigned xn0 = xoff[0] + sn, w0n = woff0 + sn, w1n = woff1 + sn; \
    KSTEP(FA, FB, x1, 1, 0, MORE) \
    KSTEP(FB, FA, x2, 2, 2, MORE) \
    KSTEP(FA, FB, x3, 3, 4, MORE) \
    WAIT_LGKM(2); SCHED(); MM1(FB, 0, 0); SCHED(); \
    if (MORE) WAIT_VM(6); else WAIT_VM(0); \
    BARRIER(); \
    SCHED(); \
    RD_X(FA, xn0); \
    MM1(FB, 0, 1); RD_W(FA, 0, w0n, w1n, 0); SCHED(); \
    MM1(FB, 1, 0); RD_W(FA, 1, w0n, w1n, 0); SCHED(); \
    MM1(FB, 1, 1); SCHED(); \
    sp = so; so = sn; sn = (sn == 2 * G1_STAGE) ? 0u : sn + G1_STAGE; }
#pragma unroll 1
    for (int s = 0; s < 6; ++s) G1_BODY(true)
#pragma unroll 1
    for (int s = 6; s < 8; ++s) G1_BODY(false)
    WAIT_LGKM(0);
    MFMA_DRAIN();
    SCHED();
    STAMPAT(2); RT2(1);
    __builtin_amdgcn_s_setprio(2);
    const __amdgpu_buffer_rsrc_t rh = __builtin_amdgcn_make_buffer_rsrc(A.h, 0, L_ * F_ * 2, 0x00020000);
    const unsigned ho = (unsigned)((m0 + mrow) * F_ + n0 + 32 * wn + 8 * hh) * 2u;
#pragma unroll
    for (int m = 0; m < 2; ++m)
#pragma unroll
      for (int t = 0; t < 2; ++t) {
        u32x4 v;
#pragma unroll
        for (int j = 0; j < 4; ++j) {
          const float o0 = gelu_t(acc[t][m][2 * j]) * acc[t][m][8 + 2 * j];
          const float o1 = gelu_t(acc[t][m][2 * j + 1]) * acc[t][m][9 + 2 * j];
          v[j] = pkbf(o0, o1);
        }
        __builtin_amdgcn_raw_buffer_store_b128(v, rh, ho + (unsigned)(m * 32 * F_ + 16 * t) * 2u, 0, 16);
      }
    __builtin_amdgcn_s_setprio(0);
    RT2(2);
    BARRIER();
    hcnt = A.cnt + (CNT_H + mt) * 64;
  } else {
#pragma unroll 1
    for (int i = 0; i < 11; ++i) BARRIER();
  }
  STAMPAT(3); RT2(3);

  {
    const int ch = w >> 2, v4 = (w + 2 * ch) & 3, d = 2 * b + ch;
    LAS char* cbase = smem + ch * CV_CH;
    const unsigned cb = sbase + ch * CV_CH;
    const int r = lane & 15, g = lane >> 4;
    const int e = (-2 * r) & 7;
    const int kb = (e == 0) ? KB0 : (e == 2 ? KB2 : (e == 4 ? KB4 : KB6));
    const int ub = (r & 1) ? UB1 : UB0;
    const int ubv = (int)cb + ub;
    const int ulv = ubv + 16 * (18 * (r >> 1) + g);
    int c0 = v4 == 0 ? -1952 : (v4 == 1 ? -1056 : (v4 == 2 ? -928 : -288));
    unsigned aad = cb + (unsigned)(kb + 16 * (g - ((r + 3) >> 2)) + 2 * KZ_ + 2 * c0 - 192);
    auto boff = [&](int c) { const int c8 = c >> 3; return 16 * (c8 + 2 * (c8 >> 4) + 4); };
#define CV_B(dst, c, T) do { int a_ = ulv + boff(c) + 2304 * (T); a_ = a_ < ubv ? ubv : a_; DS_B128(dst.u, a_, 0); } while (0)
#define MF16(acc_, A_, B_) asm volatile("v_mfma_f32_16x16x32_f16 %0, %1, %2, %0" : "+v"(acc_) : "v"(A_.u), "v"(B_.u))
    f32x4 a0[4] = {}, a1[4] = {};
    if (v4 < 3) {
      const int T = (v4 == 2) ? 0 : 1;
      const int nit = v4 == 0 ? 7 : (v4 == 1 ? 6 : 5);
      Frag W0, W1, W2, W3, B0, B1, B2, B3;
      asm volatile("" : "+v"(a1[0]), "+v"(a1[1]), "+v"(a1[2]), "+v"(a1[3]));
      DS_B128(W1.u, aad, 0); DS_B128(W2.u, aad, 64); DS_B128(W3.u, aad, 128);
      CV_B(B0, c0, T); DS_B128(W0.u, aad, 192); CV_B(B1, c0 + 32, T);
      SCHED();
#define CV_STEP(F3, F2, F1, F0, BC, BN, k) \
      WAIT_LGKM(2); SCHED(); MF16(a1[3], F3, BC); DS_B128(F3.u, aad, 256 + 64 * (k)); CV_B(BN, c0 + 32 * (k) + 64, T); SCHED(); \
      MF16(a1[2], F2, BC); MF16(a1[1], F1, BC); WAIT_LGKM(3); SCHED(); MF16(a1[0], F0, BC); SCHED();
#pragma unroll 1
      for (int it = 0; it < nit; ++it) {
        CV_STEP(W1, W2, W3, W0, B0, B2, 0)
        CV_STEP(W2, W3, W0, W1, B1, B3, 1)
        CV_STEP(W3, W0, W1, W2, B2, B0, 2)
        CV_STEP(W0, W1, W2, W3, B3, B1, 3)
        c0 += 128; aad += 256;
        if (it == 1 && has_tile) {
          WAIT_VM(0);
          if (lane == 0) __hip_atomic_fetch_add((GAS unsigned*)hcnt, 1u, __ATOMIC_RELAXED, __HIP_MEMORY_SCOPE_AGENT);
        }
      }
      WAIT_LGKM(0);
      MFMA_DRAIN();
      SCHED();
    } else {
      Frag W0, W1, W2, W3, B0, B1, B2, B3;
      asm volatile("" : "+v"(a1[0]), "+v"(a1[1]), "+v"(a1[2]), "+v"(a1[3]), "+v"(a0[0]), "+v"(a0[1]), "+v"(a0[2]), "+v"(a0[3]));
      DS_B128(W1.u, aad, 0); DS_B128(W2.u, aad, 64); DS_B128(W3.u, aad, 128);
      CV_B(B0, c0, 0); CV_B(B2, c0, 1); DS_B128(W0.u, aad, 192);
      SCHED();
#define CV_STEP2(F3, F2, F1, F0, BC, CC, BN, CN, k) \
      WAIT_LGKM(1); SCHED(); MF16(a0[3], F3, BC); MF16(a1[3], F3, CC); \
      CV_B(BN, c0 + 32 * (k) + 32, 0); CV_B(CN, c0 + 32 * (k) + 32, 1); DS_B128(F3.u, aad, 256 + 64 * (k)); SCHED(); \
      MF16(a0[2], F2, BC); MF16(a1[2], F2, CC); MF16(a0[1], F1, BC); MF16(a1[1], F1, CC); \
      WAIT_LGKM(3); SCHED(); MF16(a0[0], F0, BC); MF16(a1[0], F0, CC); SCHED();
#pragma unroll 1
      for (int it = 0; it < 3; ++it) {
        CV_STEP2(W1, W2, W3, W0, B0, B2, B1, B3, 0)
        CV_STEP2(W2, W3, W0, W1, B1, B3, B0, B2, 1)
        CV_STEP2(W3, W0, W1, W2, B0, B2, B1, B3, 2)
        CV_STEP2(W0, W1, W2, W3, B1, B3, B0, B2, 3)
        c0 += 128; aad += 256;
        if (it == 0 && has_tile) {
          WAIT_VM(0);
          if (lane == 0) __hip_atomic_fetch_add((GAS unsigned*)hcnt, 1u, __ATOMIC_RELAXED, __HIP_MEMORY_SCOPE_AGENT);
        }
      }
      WAIT_LGKM(0); SCHED();
      MF16(a0[3], W1, B0); MF16(a1[3], W1, B2);
      MFMA_DRAIN();
      SCHED();
    }
    RT2(4);
    LAS float* xch = (LAS float*)(smem + CV_XCH + ch * 12288);
    auto put = [&](int slot, const f32x4& p0, const f32x4& p1) {
      *(LAS f32x4*)(xch + slot * 512 + lane * 4) = p0; *(LAS f32x4*)(xch + slot * 512 + 256 + lane * 4) = p1; };
    if (v4 == 0) put(0, a1[2], a1[3]);
    else if (v4 == 1) put(1, a1[0], a1[1]);
    else if (v4 == 2) put(4, a1[2], a1[3]);
    else { put(2, a1[0], a1[1]); put(3, a1[2], a1[3]); put(5, a0[0], a0[1]); }
    BARRIER();
    RT2(5);
    f32x4 y0, y1;
    {
      const int s1 = v4 == 0 ? 1 : (v4 == 1 ? 0 : (v4 == 2 ? 5 : 4));
      y0 = v4 == 3 ? a0[2] : ((v4 & 1) ? a1[2] : a1[0]);
      y1 = v4 == 3 ? a0[3] : ((v4 & 1) ? a1[3] : a1[1]);
      y0 += *(LAS f32x4*)(xch + s1 * 512 + lane * 4); y1 += *(LAS f32x4*)(xch + s1 * 512 + 256 + lane * 4);
      if (v4 < 2) { y0 += *(LAS f32x4*)(xch + (2 + v4) * 512 + lane * 4); y1 += *(LAS f32x4*)(xch + (2 + v4) * 512 + 256 + lane * 4); }
    }
    const float ki = *(LAS float*)cbase;
    const float dk = bf2f(A.dsk[d]);
    const int Tf = v4 < 2 ? 1 : 0, t0 = 2 * (v4 & 1);
    const int l1 = 8 * Tf + (r >> 1), sft = r & 1;
    const __amdgpu_buffer_rsrc_t rs = __builtin_amdgcn_make_buffer_rsrc(A.sT, 0, D_ * L_ * 2, 0x00020000);
#pragma unroll
    for (int tt = 0; tt < 2; ++tt) {
      const f32x4 y = tt ? y1 : y0;
      const float send0 = sft ? y[0] : y[2], send1 = sft ? y[1] : y[3];
      const float rcv0 = __builtin_bit_cast(float, __builtin_amdgcn_update_dpp(0, __builtin_bit_cast(int, send0), 0xB1, 0xf, 0xf, true));
      const float rcv1 = __builtin_bit_cast(float, __builtin_amdgcn_update_dpp(0, __builtin_bit_cast(int, send1), 0xB1, 0xf, 0xf, true));
      float z[4];
      z[0] = sft ? rcv0 : y[0]; z[1] = sft ? y[2] : rcv0; z[2] = sft ? rcv1 : y[1]; z[3] = sft ? y[3] : rcv1;
      const int l = 128 * l1 + 32 * (t0 + tt) + 8 * g + 4 * sft;
      const int c = l >> 3;
      const u32x2 uu = *(LAS u32x2*)(cbase + UB0 + 16 * (c + 2 * (c >> 4) + 4) + 8 * sft);
      float o[4];
#pragma unroll
      for (int i = 0; i < 4; ++i) {
        const unsigned hw = (i & 1) ? (uu[i >> 1] >> 16) : (uu[i >> 1] & 0xffffu);
        const float uf = (float)__builtin_bit_cast(_Float16, (u16)hw);
        o[i] = bfr(z[i] * ki) + bfr(uf * dk);
      }
      __builtin_amdgcn_raw_buffer_store_b64((u32x2){pkbf(o[0], o[1]), pkbf(o[2], o[3])}, rs, (unsigned)(d * L_ + l) * 2u, 0, 16);
    }
    RT2(6);
    BARRIER();
  }
  STAMPAT(4); RT2(7);

  constexpr int NST = F_ / 64;
  unsigned* scnt = A.cnt + (CNT_S + (b >> 5)) * 64;
  if (w >= 4) {
    const int j = w - 4;
    const char* abase = (const char*)(A.h + (size_t)g2m0 * F_);
    unsigned aoff[2];
#pragma unroll
    for (int i = 0; i < 2; ++i) {
      const int r = 8 * (2 * j + i) + (lane >> 3);
      aoff[i] = (unsigned)(r * F_ + 8 * ((lane & 7) ^ ((r >> 1) & 7))) * 2u;
    }
#define G2_LDA(s) do { glds16_sc1(abase + (s) * 128 + aoff[0], smem + ((s) % G2_NA) * 8192 + (2 * j) * 1024); \
                       glds16_sc1(abase + (s) * 128 + aoff[1], smem + ((s) % G2_NA) * 8192 + (2 * j + 1) * 1024); } while (0)
#pragma unroll
    for (int s = 0; s < G2_NA; ++s) G2_LDA(s);
    WAIT_VM(14);
    if (lane == 0) __hip_atomic_fetch_add((GAS unsigned*)scnt, 1u, __ATOMIC_RELAXED, __HIP_MEMORY_SCOPE_AGENT);
    BARRIER();
#pragma unroll
    for (int s = 0; s < NST - 1; ++s) {
      int n = 0;
      if (s + 1 < G2_NA) n = 2 * (G2_NA - 1 - (s + 1)) + 2 * s;
      else { for (int jj = s + 2 - G2_NA; jj < s; ++jj) n += (jj + G2_NA < NST ? 2 : 0); }
      WAIT_VMN(n);
      BARRIER();
      if (s + G2_NA < NST) G2_LDA(s + G2_NA);
    }
    return;
  }
  {
    const int wm = w >> 1, wn = w & 1;
    const int mrow = 32 * wm + (lane & 31), hh = lane >> 5;
    unsigned xoff[4];
#pragma unroll
    for (int ks = 0; ks < 4; ++ks) xoff[ks] = sbase + mrow * 128 + (((2 * ks + hh) ^ ((mrow >> 1) & 7)) << 4);
    const int g4 = (lane >> 4) & 1, q = (lane & 15) >> 2, p = lane & 3;
    const unsigned woff = sbase + G2_BR + (8 * hh + q) * 128 + (((2 * wn + (p & 1)) ^ (q & 2)) * 32) + 8 * (2 * g4 + (p >> 1));
    f32x16 acc = {};
    struct G2Frags { Frag x[4]; Frag w[4]; } FA, FB;
#define G2_RD(S, sa, sb) do { _Pragma("unroll") for (int ks = 0; ks < 4; ++ks) { \
      DS_B128(S.x[ks].u, xoff[ks] + (sa), 0); DS_TR(S.w[ks].s.a, woff + (sb), ks * 2048); DS_TR(S.w[ks].s.b, woff + (sb), ks * 2048 + 512); } } while (0)
#define G2_MM(S) do { _Pragma("unroll") for (int ks = 0; ks < 4; ++ks) MFMA32_BF16(acc, S.w[ks].u, S.x[ks].u); } while (0)
#define G2_RD1(S, ks, sa, sb) do { DS_B128(S.x[ks].u, xoff[ks] + (sa), 0); DS_TR(S.w[ks].s.a, woff + (sb), ks * 2048); DS_TR(S.w[ks].s.b, woff + (sb), ks * 2048 + 512); } while (0)
#define G2_IT(CUR, NXT) do { \
    BARRIER(); SCHED(); \
    MFMA32_BF16(acc, CUR.w[0].u, CUR.x[0].u); G2_RD1(NXT, 0, an, bn); SCHED(); \
    MFMA32_BF16(acc, CUR.w[1].u, CUR.x[1].u); G2_RD1(NXT, 1, an, bn); SCHED(); \
    MFMA32_BF16(acc, CUR.w[2].u, CUR.x[2].u); G2_RD1(NXT, 2, an, bn); SCHED(); \
    MFMA32_BF16(acc, CUR.w[3].u, CUR.x[3].u); G2_RD1(NXT, 3, an, bn); SCHED(); \
    an = (an == (G2_NA - 1) * 8192) ? 0u : an + 8192; bn = (bn == (G2_NB - 1) * 8192) ? 0u : bn + 8192; } while (0)
    const size_t go = (size_t)(g2m0 + mrow) * D_ + g2n0 + 32 * wn + 16 * hh;
    u32x4 xa, xb;
    asm volatile("global_load_dwordx4 %0, %2, off\n\tglobal_load_dwordx4 %1, %2, off offset:16" : "=&v"(xa), "=&v"(xb) : "v"(A.x + go) : "memory");
    BARRIER(); SCHED();
    RT2(8);
    G2_RD(FA, 0u, 0u);
    WAIT_VM(2);
    if (lane == 0) __hip_atomic_fetch_add((GAS unsigned*)scnt, 1u, __ATOMIC_RELAXED, __HIP_MEMORY_SCOPE_AGENT);
    SCHED();
    unsigned an = 8192, bn = 8192;
#pragma unroll 1
    for (int s = 0; s < 26; s += 2) { G2_IT(FA, FB); G2_IT(FB, FA); }
    G2_IT(FA, FB);
    WAIT_LGKM(0); SCHED();
    G2_MM(FB);
    MFMA_DRAIN();
    SCHED();
    STAMPAT(5); RT2(9);
    asm volatile("s_waitcnt vmcnt(0)" : "+v"(xa), "+v"(xb) :: "memory");
    BARRIER();
    float o[16];
#pragma unroll
    for (int a = 0; a < 4; ++a) {
      const s16x4 sv = trread(smem + ST_OFF + (32 * wn + 16 * hh + 4 * a + q) * 144 + (32 * wm + 16 * g4 + 4 * p) * 2);
#pragma unroll
      for (int i = 0; i < 4; ++i) {
        const int e = 4 * a + i;
        const float ov = bfr(acc[e]);
        const float gv = bfr(ov * __builtin_amdgcn_rcpf(1.0f + __builtin_amdgcn_exp2f(-1.4426950408889634f * ov)));
        const float sf = bf2f((unsigned)(u16)sv[i]);
        const unsigned xw = e < 8 ? xa[e >> 1] : xb[(e - 8) >> 1];
        const float xf = (e & 1) ? bfhi(xw) : bflo(xw);
        o[e] = xf + bfr(sf * gv);
      }
    }
    u32x4 va, vb;
#pragma unroll
    for (int j = 0; j < 4; ++j) { va[j] = pkbf(o[2 * j], o[2 * j + 1]); vb[j] = pkbf(o[8 + 2 * j], o[9 + 2 * j]); }
    *(u32x4*)(A.out + go) = va;
    *(u32x4*)(A.out + go + 8) = vb;
    STAMPAT(6);
    STAMPAT(7); STAMPRT(1, 1); RT2(10);
  }
}

#if defined(__HIP_DEVICE_COMPILE__)
#pragma clang attribute pop
#endif

extern "C" void kernel_launch(void* const* d_in, const int* in_sizes, int n_in, void* d_out, int out_size,
                              void* d_ws, size_t ws_size, hipStream_t stream) {
  char* ws = (char*)d_ws;
  KFArgs a;
  a.x = (const u16*)d_in[0]; a.dsk = (const u16*)d_in[6]; a.prm = (const float*)(ws + OFF_KR);
  a.wg = (const u16*)d_in[8]; a.wu = (const u16*)d_in[9]; a.wd = (const u16*)d_in[10];
  a.xn = (const u16*)(ws + OFF_XN); a.uT = (const u16*)(ws + OFF_UT);
  a.h = (u16*)(ws + OFF_H); a.sT = (u16*)(ws + OFF_ST); a.cnt = (unsigned*)(ws + OFF_CNT); a.out = (u16*)d_out;
  (void)hipFuncSetAttribute((const void*)kf_main, hipFuncAttributeMaxDynamicSharedMemorySize, KF_LDS);
  k0_ln<<<256, 512, 0, stream>>>(a.x, (u16*)(ws + OFF_XN), (u16*)(ws + OFF_UT), a.cnt, (const u16*)d_in[1], (const u16*)d_in[2],
                                 (const u16*)d_in[3], (const u16*)d_in[4], (const u16*)d_in[5], (float*)(ws + OFF_KR), a.wg, a.wu);
  kf_main<<<256, 1024, KF_LDS, stream>>>(a);
}
```

```cpp
#include <hip/hip_runtime.h>
#include <hip/hip_bf16.h>
#include <stdint.h>

#if defined(__HIP_DEVICE_COMPILE__)
#pragma clang attribute push(__attribute__((target("no-packed-fp32-ops"))), apply_to = function)
#endif

#define U2F(x) __builtin_bit_cast(float, (unsigned)(x))
#define F2U(x) __builtin_bit_cast(unsigned, (float)(x))
#define TIDX ((int)__builtin_amdgcn_workitem_id_x())
#define BIDX ((int)__builtin_amdgcn_workgroup_id_x())
#define SYNCTHREADS() do { __builtin_amdgcn_fence(__ATOMIC_RELEASE, "workgroup"); __builtin_amdgcn_s_barrier(); __builtin_amdgcn_fence(__ATOMIC_ACQUIRE, "workgroup"); } while (0)

typedef unsigned short u16;
typedef __attribute__((ext_vector_type(4))) short s16x4;
typedef __attribute__((ext_vector_type(8))) __bf16 bf16x8;
typedef __attribute__((ext_vector_type(8))) _Float16 f16x8;
typedef __attribute__((ext_vector_type(2))) _Float16 f16x2;
typedef __attribute__((ext_vector_type(2))) __fp16 h16x2;
typedef __attribute__((ext_vector_type(4))) float f32x4;
typedef __attribute__((ext_vector_type(16))) float f32x16;
typedef __attribute__((ext_vector_type(4))) unsigned u32x4;
typedef __attribute__((ext_vector_type(2))) unsigned u32x2;
#define LAS __attribute__((address_space(3)))
#define GAS __attribute__((address_space(1)))

constexpr int L_ = 2048, D_ = 512, N_ = 128, F_ = 1792;
#define HP_ 1856
#define XP_ 576
constexpr int KR_ = 2240;
constexpr int KZ_ = 2080;

constexpr size_t OFF_XN = 0;
constexpr size_t OFF_UT = 20u << 20;
constexpr size_t OFF_KR = 4u << 20;
constexpr size_t OFF_KINV = 7u << 20;
constexpr size_t OFF_CNT = (7u << 20) + 65536;
constexpr size_t OFF_H = 8u << 20;
constexpr size_t OFF_ST = 16u << 20;

__device__ __forceinline__ float bf2f(unsigned v) { return U2F(v << 16); }
__device__ __forceinline__ float bflo(unsigned v) { return U2F(v << 16); }
__device__ __forceinline__ float bfhi(unsigned v) { return U2F(v & 0xffff0000u); }
__device__ __forceinline__ float bfr(float f) { return (float)(__bf16)f; }
__device__ __forceinline__ unsigned pkbf(float a, float b) {
  typedef __attribute__((ext_vector_type(2))) __bf16 bf16x2;
  bf16x2 v; v.x = (__bf16)a; v.y = (__bf16)b;
  return __builtin_bit_cast(unsigned, v);
}
__device__ __forceinline__ unsigned pkh(float a, float b) {
  f16x2 v; v.x = (_Float16)a; v.y = (_Float16)b;
  return __builtin_bit_cast(unsigned, v);
}
__device__ __forceinline__ void glds16(const void* g, LAS void* l) {
  __builtin_amdgcn_global_load_lds((const GAS void*)g, l, 16, 0, 0);
}
__device__ __forceinline__ void glds16_sc1(const void* g, LAS void* l) {
  __builtin_amdgcn_global_load_lds((const GAS void*)g, l, 16, 0, 16);
}
__device__ __forceinline__ s16x4 trread(LAS char* p) {
  return __builtin_amdgcn_ds_read_tr16_b64_v4i16((LAS s16x4*)p);
}
union Frag { struct { s16x4 a, b; } s; bf16x8 bf; f16x8 h; u32x4 u; };

#define WAIT_VM(n) asm volatile("s_waitcnt vmcnt(" #n ")" ::: "memory")
#define WAIT_VMN(n) asm volatile("s_waitcnt vmcnt(%0)" :: "i"(n) : "memory")
#define BARRIER() do { asm volatile("s_waitcnt lgkmcnt(0)" ::: "memory"); __builtin_amdgcn_s_barrier(); asm volatile("" ::: "memory"); } while (0)

#define DS_TR(dst, addr, off) asm volatile("ds_read_b64_tr_b16 %0, %1 offset:%2" : "=v"(dst) : "v"(addr), "i"(off))
#define DS_B128(dst, addr, off) asm volatile("ds_read_b128 %0, %1 offset:%2" : "=v"(dst) : "v"(addr), "i"(off))
#define WAIT_LGKM(n) asm volatile("s_waitcnt lgkmcnt(" #n ")" ::: "memory")
#define SCHED() __builtin_amdgcn_sched_barrier(0)
#define MFMA32_BF16(acc, a, b) asm volatile("v_mfma_f32_32x32x16_bf16 %0, %1, %2, %0" : "+v"(acc) : "v"(a), "v"(b))
#define MFMA_DRAIN() asm volatile("s_nop 15\n\ts_nop 7" ::: "memory")
#define STAMPAT(i) do { } while (0)
#define STAMPRT(k, i) do { } while (0)
#define RT2(i) do { } while (0)

__device__ __forceinline__ float wave_sum(float v) {
  v += __builtin_bit_cast(float, __builtin_amdgcn_update_dpp(0, __builtin_bit_cast(int, v), 0xB1, 0xf, 0xf, true));
  v += __builtin_bit_cast(float, __builtin_amdgcn_update_dpp(0, __builtin_bit_cast(int, v), 0x4E, 0xf, 0xf, true));
  v += __builtin_bit_cast(float, __builtin_amdgcn_update_dpp(0, __builtin_bit_cast(int, v), 0x141, 0xf, 0xf, true));
  v += __builtin_bit_cast(float, __builtin_amdgcn_update_dpp(0, __builtin_bit_cast(int, v), 0x140, 0xf, 0xf, true));
  const float r0 = __builtin_bit_cast(float, __builtin_amdgcn_readlane(__builtin_bit_cast(int, v), 0));
  const float r1 = __builtin_bit_cast(float, __builtin_amdgcn_readlane(__builtin_bit_cast(int, v), 16));
  const float r2 = __builtin_bit_cast(float, __builtin_amdgcn_readlane(__builtin_bit_cast(int, v), 32));
  const float r3 = __builtin_bit_cast(float, __builtin_amdgcn_readlane(__builtin_bit_cast(int, v), 48));
  return (r0 + r1) + (r2 + r3);
}
__device__ __forceinline__ void cexp_p(float arl2, float wh, float wl, float p, float& re, float& im) {
  const float mag = __builtin_amdgcn_exp2f(arl2 * p);
  const float fr = __builtin_fmaf(wl, p, __builtin_amdgcn_fractf(wh * p));
  re = mag * __builtin_amdgcn_cosf(fr);
  im = mag * __builtin_amdgcn_sinf(fr);
}
__device__ __forceinline__ void split_h(float a, float b, unsigned& hi, unsigned& lo) {
  h16x2 h = __builtin_amdgcn_cvt_pkrtz(a, b);
  float ra = a - (float)h.x, rb = b - (float)h.y;
  h16x2 l = __builtin_amdgcn_cvt_pkrtz(ra, rb);
  hi = __builtin_bit_cast(unsigned, h);
  lo = __builtin_bit_cast(unsigned, l);
}

constexpr int G1_A = 16384, G1_W = 32768, G1_STAGE = G1_A + G1_W;
constexpr int KF_LDS = 163840;
#define HSWAP 0
constexpr int HS_PART = 147456;
constexpr int HS_PRM = HS_PART + 4 * 2816;
constexpr int CV_KS = 4672, CV_US = 4736, CV_CH = 4 * CV_KS + 2 * CV_US;
constexpr int KB2 = 64, KB4 = 64 + CV_KS, KB6 = 64 + 2 * CV_KS, KB0 = 64 + 3 * CV_KS - 16;
constexpr int UB0 = 4 * CV_KS + 64, UB1 = UB0 + CV_US;
constexpr int CV_XCH = 57344;
constexpr int CV_KL = 73728;
constexpr int G2_BR = 81920, G2_NB = 10, G2_NA = 8;
constexpr int ST_OFF = 5 * 8192;
constexpr int FL_LN = 0, FL_H = 2048, FL_S = 4096, FL_U = 6144;
constexpr int LN_TILE = 2 * G1_STAGE;

__device__ __forceinline__ float gelu_t(float x) {
  const float c1 = -2.0f * 0.7978845608028654f * 1.4426950408889634f;
  const float c2 = c1 * 0.044715f;
  float a = x * __builtin_fmaf(x * x, c2, c1);
  float e = __builtin_amdgcn_exp2f(a);
  return x * __builtin_amdgcn_rcpf(1.0f + e);
}
typedef unsigned long long u64;
extern "C" __device__ u64 kf_dispatch_id() __asm("llvm.amdgcn.dispatch.id");
__device__ __forceinline__ u64 launch_epoch() {
  const u64 x = kf_dispatch_id() * 0x9E3779B97F4A7C15ull + (u64)(size_t)__builtin_amdgcn_dispatch_ptr() * 0xC2B2AE3D27D4EB4Full;
  return (x ^ (x >> 29)) | 0x8000000000000001ull;
}
__device__ __forceinline__ void set_flag(u64* p, u64 e) { __hip_atomic_store((GAS u64*)p, e, __ATOMIC_RELAXED, __HIP_MEMORY_SCOPE_AGENT); }
__device__ __forceinline__ void wait_flags2(const u64* p0, const u64* p1, u64 e) {
  for (unsigned spins = 0; spins < (1u << 20); ++spins) {
    const u64 a = __hip_atomic_load((GAS u64*)p0, __ATOMIC_RELAXED, __HIP_MEMORY_SCOPE_AGENT);
    const u64 b = __hip_atomic_load((GAS u64*)p1, __ATOMIC_RELAXED, __HIP_MEMORY_SCOPE_AGENT);
    if (__builtin_amdgcn_ballot_w64(a != e || b != e) == 0ull) break;
    __builtin_amdgcn_s_sleep(1);
  }
}

struct KIdx { u16 v[2304]; };
constexpr KIdx make_kidx() {
  KIdx t{};
  for (int x = 0; x < 2304; ++x) {
    const int idx = KZ_ - x;
    int o = 21 * 32 + 1;
    if (idx >= 0 && idx < L_) {
      int p = idx;
      int sh = idx >= 1024 ? 3 : (idx >= 512 ? 2 : (idx >= 256 ? 1 : 0));
      if (sh) {
        const int q = idx >> sh, rem = idx & ((1 << sh) - 1), half = 1 << (sh - 1);
        p = (q + ((rem > half || (rem == half && (q & 1))) ? 1 : 0)) << sh;
      }
      const int r = p & 7, cc = p >> 3, c1 = cc >> 5, c0 = cc & 31;
      const int rw = c1 == 0 ? r : (c1 == 1 ? 8 + (r >> 1) : (c1 < 4 ? 8 + 2 * c1 + (r >> 2) : 12 + c1));
      o = rw * 32 + c0;
    }
    t.v[x] = (u16)(o * 4);
  }
  return t;
}
__device__ const KIdx g_kidx = make_kidx();

struct G1Frags { Frag x[2]; Frag w[2]; };
struct KFArgs {
  const u16 *x, *lAr, *Aim, *Cr, *Ci, *ldt, *dsk, *wg, *wu, *wd;
  u16 *xn, *uT, *h, *sT; u64* fl; u16* out;
};

__device__ __forceinline__ unsigned mov32(unsigned x) { unsigned r; asm("v_mov_b32 %0, %1" : "=v"(r) : "v"(x)); return r; }
__device__ __forceinline__ void split_h2(float a, float b, unsigned& hi, unsigned& lo) {
  const h16x2 h = __builtin_amdgcn_cvt_pkrtz(a, b);
  f16x2 l;
  l.x = (_Float16)__builtin_fmaf((float)h.x, -1.0f, a);
  l.y = (_Float16)__builtin_fmaf((float)h.y, -1.0f, b);
  hi = __builtin_bit_cast(unsigned, h);
  lo = __builtin_bit_cast(unsigned, l);
}
#define HBAR() do { SCHED(); asm volatile("" ::: "memory"); __builtin_amdgcn_s_barrier(); asm volatile("" ::: "memory"); SCHED(); } while (0)
__device__ __forceinline__ void kf_helper(LAS char* smem, const KFArgs& A, int j, int lane, int g2m0, int g2n0, u64 ep, int tmt, int tkb) {
  const int b = BIDX, ch = j >> 2, nq = j & 3, d = 2 * b + ch;
  LAS float* prm = (LAS float*)(smem + HS_PRM) + j * 160;
  LAS float* part = (LAS float*)(smem + HS_PART) + (j >> 1) * 704;
  const int uc = 64 * nq + lane;
  unsigned warm = 0, warm2 = 0;
  const int ht = j * 64 + lane, bi = b >> 3;
#define TOUCH(reg, ptr) asm volatile("global_load_dword %0, %1, off" : "+v"(reg) : "v"(ptr) : "memory")
  const u32x4 kx0 = *(const u32x4*)(g_kidx.v + 8 * (4 + uc));
  const int c2 = lane < 4 ? lane : 256 + (lane < 21 ? lane : 20);
  const u32x4 kx1 = *(const u32x4*)(g_kidx.v + 8 * c2);
  float kinv;
  {
    const int n = d * N_ + nq * 32 + (lane & 31);
    const unsigned vdt = A.ldt[d];
    const unsigned a = *(const unsigned*)(A.Cr + d * N_ + 2 * lane), bb = *(const unsigned*)(A.Ci + d * N_ + 2 * lane);
    const unsigned v0 = A.lAr[n], v1 = A.Aim[n], v2 = A.Cr[n], v3 = A.Ci[n];
    const float dt = bfr(__builtin_expf(bf2f(vdt)));
    const float sb = wave_sum((__builtin_fabsf(bflo(a)) + __builtin_fabsf(bfhi(a))) + (__builtin_fabsf(bflo(bb)) + __builtin_fabsf(bfhi(bb)))) * dt * 2.0f;
    const int ex = (int)((F2U(sb) >> 23) & 0xff) - 127;
    int se = 14 - ex;
    se = se > 100 ? 100 : (se < -100 ? -100 : se);
    const float cs = dt * 2.0f * U2F((unsigned)(127 + se) << 23);
    kinv = U2F((unsigned)(127 - se) << 23);
    const float ar = -bfr(__builtin_expf(bf2f(v0))) * dt;
    const float ai = bf2f(v1) * dt;
    const float w0 = ai * 0.15915494309189535f;
    const float wh = U2F(F2U(w0) & 0xfffff000u);
    const float wl = __builtin_fmaf(ai, 6.4206383e-09f, __builtin_fmaf(ai, 0.15915494309189535f, -wh));
    if (lane < 32) {
      prm[lane] = ar * 1.4426950408889634f; prm[32 + lane] = wh; prm[64 + lane] = wl;
      prm[96 + lane] = bf2f(v2) * cs; prm[128 + lane] = bf2f(v3) * cs;
    }
  }
  if ((j & 1) == 0 && lane == 0) part[21 * 32 + 1] = 0.f;
  const int row = lane & 31, hh = lane >> 5;
  int prow;
  if (row < 8) prow = row;
  else if (row < 12) prow = 256 + 2 * (row - 8);
  else if (row < 16) prow = 512 + 256 * ((row - 12) >> 1) + 4 * ((row - 12) & 1);
  else prow = 1024 + 256 * (row - 16);
  prow = row < 21 ? prow : 0;
  const float pA = (float)prow, pB = (float)(8 * row);
  f32x16 acc = {};
  const int pnl = 4 * nq + ((lane & 7) < 4 ? (lane & 7) : 4);
  const u64* f0 = A.fl + FL_U + (pnl < 16 ? pnl : 15) * 64 + (d >> 6) * 8 + ((d >> 4) & 3) + 4 * ((lane >> 3) & 1);
  u64 fa = 0;
#pragma unroll 1
  for (int ks = 0; ks < 4; ++ks) {
    if (ks == 1 && tkb >= 0) {
      const __amdgpu_buffer_rsrc_t ru = __builtin_amdgcn_make_buffer_rsrc(A.uT, 0, L_ * D_ * 2, 0x00020000);
      const int q = (lane & 15) >> 2, p = lane & 3, chk = 2 * (j & 3) + (p >> 1);
#pragma unroll
      for (int t = 0; t < 2; ++t) {
        const int r0 = 64 * (j >> 2) + 32 * t + 8 * (lane >> 4);
        const int ra = r0 + q, rb = r0 + 4 + q;
        const s16x4 va_ = trread(smem + ra * 128 + ((chk ^ ((ra >> 1) & 7)) << 4) + 8 * (p & 1));
        const s16x4 vb_ = trread(smem + rb * 128 + ((chk ^ ((rb >> 1) & 7)) << 4) + 8 * (p & 1));
        const u32x2 ua_ = __builtin_bit_cast(u32x2, va_), ub_ = __builtin_bit_cast(u32x2, vb_);
        u32x4 o;
        o[0] = __builtin_bit_cast(unsigned, __builtin_amdgcn_cvt_pkrtz(bflo(ua_[0]), bfhi(ua_[0])));
        o[1] = __builtin_bit_cast(unsigned, __builtin_amdgcn_cvt_pkrtz(bflo(ua_[1]), bfhi(ua_[1])));
        o[2] = __builtin_bit_cast(unsigned, __builtin_amdgcn_cvt_pkrtz(bflo(ub_[0]), bfhi(ub_[0])));
        o[3] = __builtin_bit_cast(unsigned, __builtin_amdgcn_cvt_pkrtz(bflo(ub_[1]), bfhi(ub_[1])));
        __builtin_amdgcn_raw_buffer_store_b128(o, ru, (unsigned)((64 * tkb + 16 * (j & 3) + (lane & 15)) * L_ + 128 * tmt + r0) * 2u, 0, 16);
      }
    }
    if (ks == 2 && tkb >= 0) {
      WAIT_VM(0);
      if (lane == 0) set_flag(A.fl + FL_U + (tmt * 8 + tkb) * 8 + j, ep);
    }
    const int n0 = ks * 8 + hh * 4;
    const f32x4 va = *(LAS f32x4*)(prm + n0), vh = *(LAS f32x4*)(prm + 32 + n0), vl = *(LAS f32x4*)(prm + 64 + n0);
    const f32x4 vr = *(LAS f32x4*)(prm + 96 + n0), vi = *(LAS f32x4*)(prm + 128 + n0);
    Frag Ah, Al, Bh, Bl;
#pragma unroll
    for (int q = 0; q < 4; ++q) {
      float er, ei, yr, yi;
      cexp_p(va[q], vh[q], vl[q], pA, er, ei);
      cexp_p(va[q], vh[q], vl[q], pB, yr, yi);
      const float xr = vr[q] * er - vi[q] * ei, xi = vr[q] * ei + vi[q] * er;
      unsigned h0, l0, h1, l1;
      split_h(xr, xi, h0, l0);
      split_h(yr, -yi, h1, l1);
      Ah.u[q] = h0; Al.u[q] = l0; Bh.u[q] = h1; Bl.u[q] = l1;
      if (q == 1 && ks > 0) {
        asm volatile("s_waitcnt lgkmcnt(0)" : "+v"(Ah.u), "+v"(Al.u), "+v"(Bh.u), "+v"(Bl.u) :: "memory");
        HBAR();
      }
    }
    acc = __builtin_amdgcn_mfma_f32_32x32x16_f16(Al.h, Bh.h, acc, 0, 0, 0);
    acc = __builtin_amdgcn_mfma_f32_32x32x16_f16(Ah.h, Bl.h, acc, 0, 0, 0);
    acc = __builtin_amdgcn_mfma_f32_32x32x16_f16(Ah.h, Bh.h, acc, 0, 0, 0);
    RT2(1 + ks);
    if (ks == 3) asm volatile("global_load_dwordx2 %0, %1, off sc1" : "+v"(fa) : "v"(f0) : "memory");
    HBAR();
  }
  if ((j & 1) == 0) {
#pragma unroll
    for (int i = 0; i < 12; ++i) {
      const int rr = (i & 3) + 8 * (i >> 2) + 4 * hh;
      if (rr < 21) part[rr * 32 + row] = acc[i];
    }
  }
  RT2(5); BARRIER();
  if (j & 1) {
#pragma unroll
    for (int i = 0; i < 12; ++i) {
      const int rr = (i & 3) + 8 * (i >> 2) + 4 * hh;
      if (rr < 21) part[rr * 32 + row] += acc[i];
    }
  }
  asm volatile("s_waitcnt vmcnt(0)" : "+v"(fa) :: "memory");
  if (__builtin_amdgcn_ballot_w64(fa != ep) != 0ull) wait_flags2(f0, f0, ep);
  u32x4 ua = {0u, 0u, 0u, 0u}; unsigned unx = 0;
  {
    const u16* ur = A.uT + (size_t)d * L_ + 8 * uc;
    asm volatile("global_load_dwordx4 %0, %2, off sc1\n\tglobal_load_ushort %1, %3, off sc1" : "+v"(ua), "+v"(unx) : "v"(ur), "v"(ur + (uc < 255 ? 8 : 0)) : "memory");
  }
  RT2(6); BARRIER();
  {
    const LAS char* t0 = smem + HS_PART + (2 * ch) * 2816;
    LAS char* cbase = smem + ch * CV_CH;
    auto expand = [&](const u32x4& kx, int c) {
      float v[8];
#pragma unroll
      for (int e = 0; e < 8; ++e) {
        const unsigned o = (e & 1) ? (kx[e >> 1] >> 16) : (kx[e >> 1] & 0xffffu);
        v[e] = *(const LAS float*)(t0 + o) + *(const LAS float*)(t0 + o + 2816);
      }
      u32x4 o;
#pragma unroll
      for (int e = 0; e < 4; ++e) o[e] = pkh(v[2 * e], v[2 * e + 1]);
      *(LAS u32x4*)(cbase + KB0 + 16 * c) = o;
      LAS unsigned* q2 = (LAS unsigned*)(cbase + KB2 + 16 * c - 4);
      LAS u32x2* q4 = (LAS u32x2*)(cbase + KB4 + 16 * c - 8);
      LAS unsigned* q6 = (LAS unsigned*)(cbase + KB6 + 16 * c - 12);
#pragma unroll
      for (int e = 0; e < 4; ++e) { q2[e] = o[e]; q6[e] = o[e]; }
      q4[0] = (u32x2){o[0], o[1]}; q4[1] = (u32x2){o[2], o[3]};
    };
    expand(kx0, 4 + uc);
    if (nq == 0) {
      if (lane < 21) expand(kx1, c2);
      if (lane == 0) *(LAS float*)(cbase) = kinv;
    }
    asm volatile("s_waitcnt vmcnt(0)" : "+v"(ua), "+v"(unx) :: "memory");
    const unsigned nx = (uc < 255) ? unx : 0u;
    const int P = uc + 2 * (uc >> 4) + 4;
    *(LAS u32x4*)(cbase + UB0 + 16 * P) = ua;
    u32x4 s1;
    s1[0] = (ua[0] >> 16) | (ua[1] << 16); s1[1] = (ua[1] >> 16) | (ua[2] << 16);
    s1[2] = (ua[2] >> 16) | (ua[3] << 16); s1[3] = (ua[3] >> 16) | (nx << 16);
    *(LAS u32x4*)(cbase + UB1 + 16 * P) = s1;
    if (uc == 0) {
      const u32x4 z = {0u, 0u, 0u, 0u};
      *(LAS u32x4*)(cbase + UB0) = z; *(LAS u32x4*)(cbase + UB0 + 16) = z;
      *(LAS u32x4*)(cbase + UB1) = z; *(LAS u32x4*)(cbase + UB1 + 16) = (u32x4){0u, 0u, 0u, ua[0] << 16};
    }
  }
  STAMPAT(6);
  BARRIER();
  STAMPAT(7);
  {
    constexpr int NST = F_ / 64;
    const char* bbase = (const char*)(A.wd + g2n0);
    const int r = 8 * j + (lane >> 3);
    const unsigned boff = (unsigned)(r * D_) * 2u + (unsigned)((((lane & 7) >> 1) ^ (r & 2)) * 32 + (lane & 1) * 16);
    const char* bp = bbase + boff;
    unsigned slot = 0;
    TOUCH(warm, (const char*)A.wd + (size_t)(bi * 448 + (ht < 448 ? ht : 447)) * 128);
#pragma unroll 1
    for (int s = 0; s < G2_NB - 1; ++s) { glds16(bp, smem + G2_BR + slot + j * 1024); bp += 128 * D_; slot += 8192; }
    BARRIER();
    if (j == 0) { const u64* hf = A.fl + FL_H + (g2m0 >> 7) * 128; wait_flags2(hf + lane, hf + (lane < 48 ? 64 + lane : lane), ep); }
    BARRIER();
    {
      const int t = (g2n0 >> 6) * 224 + (ht < 224 ? ht : 223);
      const int ln = t >> 6, r = t & 63;
      TOUCH(warm2, (const char*)A.h + (size_t)(g2m0 + r) * (HP_ * 2) + ln * 128);
    }
    asm volatile("s_waitcnt vmcnt(%1)" : "+v"(warm) : "i"(G2_NB - 1) : "memory");
    BARRIER();
    const u64* sf = A.fl + FL_S + (g2n0 >> 6) * 256 + 32 * j + (lane & 31);
    u64 sflag = 0;
    u32x4 sv = {0u, 0u, 0u, 0u};
    const int sc = j * 64 + lane;
#pragma unroll 1
    for (int s = 0; s <= NST - G2_NB; ++s) {
      WAIT_VMN(G2_NB - 3);
      BARRIER();
      if (s == 12) asm volatile("global_load_dwordx2 %0, %1, off sc1" : "+v"(sflag) : "v"(sf) : "memory");
      glds16(bp, smem + G2_BR + slot + j * 1024);
      bp += 128 * D_;
      slot = (slot == (G2_NB - 1) * 8192) ? 0u : slot + 8192;
    }
    WAIT_VM(7); BARRIER();
    WAIT_VM(6); BARRIER();
    asm volatile("s_waitcnt vmcnt(5)" : "+v"(sflag) :: "memory");
    BARRIER();
    {
      if (__builtin_amdgcn_ballot_w64(sflag != ep) != 0ull) wait_flags2(sf, sf, ep);
      const __amdgpu_buffer_rsrc_t rs = __builtin_amdgcn_make_buffer_rsrc(A.sT, 0, D_ * L_ * 2, 0x00020000);
      const unsigned so = (unsigned)(((g2n0 + (sc >> 3)) * L_ + g2m0 + 8 * (sc & 7)) * 2);
      asm volatile("buffer_load_dwordx4 %0, %1, %2, 0 offen sc1" : "+v"(sv) : "v"(so), "s"(rs) : "memory");
    }
    WAIT_VM(5); BARRIER();
    WAIT_VM(4); BARRIER();
    WAIT_VM(3); BARRIER();
    WAIT_VM(2); BARRIER();
    WAIT_VM(1); BARRIER();
    asm volatile("s_waitcnt vmcnt(0)" : "+v"(sv), "+v"(warm2) :: "memory");
    *(LAS u32x4*)(smem + ST_OFF + (sc >> 3) * 144 + (sc & 7) * 16) = sv;
    BARRIER();
  }
}

__global__ __launch_bounds__(1024, 4) void kf_main(KFArgs A) {
  extern __shared__ __attribute__((aligned(16))) char smem_[];
  LAS char* smem = (LAS char*)smem_;
  const unsigned sbase = (unsigned)(size_t)smem;
  const int tid = TIDX, lane = tid & 63;
  const int w = __builtin_amdgcn_readfirstlane(tid >> 6);
  const int b = BIDX, xcd = b & 7, idx = b >> 3;
  const bool has_tile = idx < 28;
  const int g2mt = xcd * 4 + (idx & 3), g2nt = idx >> 2;
  const int g2m0 = g2mt * 64, g2n0 = g2nt * 64;
  STAMPAT(0); STAMPRT(1, 0); RT2(0);
  const u64 ep = launch_epoch();
  const int mt = (xcd & 3) * 4 + (idx & 3), nt = (xcd >> 2) * 7 + (has_tile ? idx >> 2 : 6);
  const int rot = nt & 7;
  if (w >= 8) { kf_helper(smem, A, w - 8, lane, g2m0, g2n0, ep, mt, (has_tile && nt < 8) ? (nt & 7) : -1); return; }

  const int m0 = mt * 128, n0 = nt * 128;
  u64* hflag = A.fl + FL_H + mt * 128 + nt * 8 + w;
  const char* abase = (const char*)(A.xn + (size_t)m0 * XP_);
  const char* gbase = (const char*)(A.wg + n0);
  const char* ubase = (const char*)(A.wu + n0);
  const int arow = 16 * w + (lane >> 3);
  const unsigned aoff0 = (unsigned)(arow * XP_ + 8 * ((lane & 7) ^ ((arow >> 1) & 7))) * 2u;
  const unsigned aoff1 = (unsigned)((arow + 8) * XP_ + 8 * ((lane & 7) ^ (((arow + 8) >> 1) & 7))) * 2u;
  const int wk = 8 * w + (lane >> 3);
  const int fk = (wk >> 1) & 1;
  const unsigned woffg = (unsigned)(wk * F_) * 2u + (unsigned)((((lane & 7) >> 1) ^ fk) * 32 + (lane & 1) * 16);
  const unsigned woffu = (unsigned)(wk * F_) * 2u + (unsigned)((((lane & 7) >> 1) ^ (fk | 2)) * 32 + (lane & 1) * 16);
#define G1_LD(i, s_, sb) do { const int s = ((s_) + rot) & 7; \
    if ((i) == 0) glds16_sc1(abase + (s) * 128 + aoff0, smem + (sb) + (2 * w) * 1024); \
    if ((i) == 1) glds16(gbase + (long)(s) * (128 * F_) + woffg, smem + (sb) + G1_A + w * 1024); \
    if ((i) == 2) glds16(gbase + (long)(s) * (128 * F_) + 128 + woffg, smem + (sb) + G1_A + 8192 + w * 1024); \
    if ((i) == 3) glds16_sc1(abase + (s) * 128 + aoff1, smem + (sb) + (2 * w + 1) * 1024); \
    if ((i) == 4) glds16(ubase + (long)(s) * (128 * F_) + woffu, smem + (sb) + G1_A + 16384 + w * 1024); \
    if ((i) == 5) glds16(ubase + (long)(s) * (128 * F_) + 128 + woffu, smem + (sb) + G1_A + 24576 + w * 1024); } while (0)
  {
    const int row = b * 8 + w;
    u32x4 v;
    asm volatile("global_load_dwordx4 %0, %1, off" : "=v"(v) : "v"(A.x + (size_t)row * D_ + lane * 8) : "memory");
    G1_LD(1, 0, 0); G1_LD(2, 0, 0); G1_LD(4, 0, 0); G1_LD(5, 0, 0);
    G1_LD(1, 1, G1_STAGE); G1_LD(2, 1, G1_STAGE); G1_LD(4, 1, G1_STAGE); G1_LD(5, 1, G1_STAGE);
    asm volatile("s_waitcnt vmcnt(8)" : "+v"(v) :: "memory");
    RT2(11);
    float f[8];
#pragma unroll
    for (int j = 0; j < 4; ++j) { f[2 * j] = bflo(v[j]); f[2 * j + 1] = bfhi(v[j]); }
    const float mean = wave_sum(((f[0] + f[1]) + (f[2] + f[3])) + ((f[4] + f[5]) + (f[6] + f[7]))) * (1.0f / D_);
    float a = 0.f;
#pragma unroll
    for (int j = 0; j < 8; ++j) { const float dd = f[j] - mean; a = __builtin_fmaf(dd, dd, a); }
    const float var = wave_sum(a) * (1.0f / D_);
    const float mb = bfr(mean);
    const float sd = bfr(__builtin_sqrtf(bfr(bfr(var) + 1e-5f)));
    const float rs = __builtin_amdgcn_rcpf(sd);
    u32x4 pb;
#pragma unroll
    for (int j = 0; j < 4; ++j) {
      const unsigned t = pkbf(f[2 * j] - mb, f[2 * j + 1] - mb);
      const unsigned o = pkbf(bflo(t) * rs, bfhi(t) * rs);
      pb[j] = o;
    }
    const __amdgpu_buffer_rsrc_t rx = __builtin_amdgcn_make_buffer_rsrc(A.xn, 0, L_ * XP_ * 2, 0x00020000);
    __builtin_amdgcn_raw_buffer_store_b128(pb, rx, (unsigned)(row * XP_ + lane * 8) * 2u, 0, 16);
    WAIT_VM(0);
    RT2(12);
    if (lane == 0) set_flag(A.fl + FL_LN + b * 8 + w, ep);
    if (has_tile) { const u64* lf = A.fl + FL_LN + mt * 128 + 16 * w + (lane & 15); wait_flags2(lf, lf, ep); }
    RT2(13); RT2(14);
  }
  if (has_tile) {
    G1_LD(0, 0, 0); G1_LD(3, 0, 0);
    G1_LD(0, 1, G1_STAGE); G1_LD(3, 1, G1_STAGE);
    const int wm = w & 1, wn = w >> 1;
    const int mrow = 64 * wm + (lane & 31), hh = lane >> 5;
    unsigned xoff[4];
#pragma unroll
    for (int ks = 0; ks < 4; ++ks) xoff[ks] = sbase + mrow * 128 + (((2 * ks + hh) ^ ((mrow >> 1) & 7)) << 4);
    const int mat = (lane >> 4) & 1, q = (lane & 15) >> 2, p = lane & 3;
    const int pp = ((p & 1) << 1) | (p >> 1);
    const int fq = ((q >> 1) & 1) | (mat << 1);
    const unsigned wrow = sbase + G1_A + (mat * 2 + (wn >> 1)) * 8192 + (8 * hh + q) * 128 + pp * 8;
    const unsigned woff0 = wrow + (((2 * (wn & 1)) ^ fq) * 32);
    const unsigned woff1 = wrow + (((2 * (wn & 1) + 1) ^ fq) * 32);
    f32x16 acc[2][2];
#pragma unroll
    for (int t = 0; t < 2; ++t) { acc[t][0] = (f32x16){}; acc[t][1] = (f32x16){}; }
    G1Frags FA, FB;
#define RD_W(S, t, b0, b1, ks) do { DS_TR(S.w[t].s.a, (t) ? b1 : b0, (ks) * 2048); DS_TR(S.w[t].s.b, (t) ? b1 : b0, (ks) * 2048 + 512); } while (0)
#define RD_X(S, XA) do { DS_B128(S.x[0].u, XA, 0); DS_B128(S.x[1].u, XA, 4096); } while (0)
#define MM1(S, t, m) MFMA32_BF16(acc[t][m], S.w[t].u, S.x[m].u)
    WAIT_VM(2);
    BARRIER();
    STAMPAT(1); RT2(15);
    RD_X(FA, xoff[0]); RD_W(FA, 0, woff0, woff1, 0); RD_W(FA, 1, woff0, woff1, 0);
    SCHED();
    unsigned so = 0, sn = G1_STAGE, sp = 2 * G1_STAGE;
#define KSTEP(CUR, NXT, XA, KSN, L0, MORE) \
    WAIT_LGKM(2); SCHED(); MM1(CUR, 0, 0); RD_X(NXT, XA); SCHED(); \
    MM1(CUR, 0, 1); RD_W(NXT, 0, w0, w1, KSN); if (MORE) G1_LD(L0, st + 2, sp); SCHED(); \
    WAIT_LGKM(4); SCHED(); MM1(CUR, 1, 0); RD_W(NXT, 1, w0, w1, KSN); SCHED(); \
    MM1(CUR, 1, 1); if (MORE) G1_LD(L0 + 1, st + 2, sp); SCHED();
#define G1_BODY(MORE) { \
    const unsigned w0 = woff0 + so, w1 = woff1 + so; \
    const unsigned x1 = xoff[1] + so, x2 = xoff[2] + so, x3 = xoff[3] + so; \
    const unsigned xn0 = xoff[0] + sn, w0n = woff0 + sn, w1n = woff1 + sn; \
    KSTEP(FA, FB, x1, 1, 0, MORE) \
    KSTEP(FB, FA, x2, 2, 2, MORE) \
    KSTEP(FA, FB, x3, 3, 4, MORE) \
    WAIT_LGKM(2); SCHED(); MM1(FB, 0, 0); SCHED(); \
    if (MORE) WAIT_VM(6); else WAIT_VM(0); \
    BARRIER(); \
    SCHED(); \
    RD_X(FA, xn0); \
    MM1(FB, 0, 1); RD_W(FA, 0, w0n, w1n, 0); SCHED(); \
    MM1(FB, 1, 0); RD_W(FA, 1, w0n, w1n, 0); SCHED(); \
    MM1(FB, 1, 1); SCHED(); \
    sp = so; so = sn; sn = (sn == 2 * G1_STAGE) ? 0u : sn + G1_STAGE; }
#pragma unroll 1
    for (int st = 0; st < 6; ++st) G1_BODY(true)
#pragma unroll 1
    for (int st = 6; st < 8; ++st) G1_BODY(false)
    WAIT_LGKM(0);
    MFMA_DRAIN();
    SCHED();
    STAMPAT(2); RT2(1);
    __builtin_amdgcn_s_setprio(2);
    const __amdgpu_buffer_rsrc_t rh = __builtin_amdgcn_make_buffer_rsrc(A.h, 0, L_ * HP_ * 2, 0x00020000);
    const unsigned ho = (unsigned)((m0 + mrow) * HP_ + n0 + 32 * wn + 8 * hh) * 2u;
#pragma unroll
    for (int m = 0; m < 2; ++m)
#pragma unroll
      for (int t = 0; t < 2; ++t) {
        u32x4 v;
#pragma unroll
        for (int j = 0; j < 4; ++j) {
          const float o0 = gelu_t(acc[t][m][2 * j]) * acc[t][m][8 + 2 * j];
          const float o1 = gelu_t(acc[t][m][2 * j + 1]) * acc[t][m][9 + 2 * j];
          v[j] = pkbf(o0, o1);
        }
        __builtin_amdgcn_raw_buffer_store_b128(v, rh, ho + (unsigned)(m * 32 * HP_ + 16 * t) * 2u, 0, 16);
      }
    __builtin_amdgcn_s_setprio(0);
    RT2(2);
    BARRIER();
  } else {
#pragma unroll 1
    for (int i = 0; i < 10; ++i) BARRIER();
  }
  STAMPAT(3); RT2(3);

  {
    const int ch = w >> 2, v4 = (w + 2 * ch) & 3, d = 2 * b + ch;
    LAS char* cbase = smem + ch * CV_CH;
    const unsigned cb = sbase + ch * CV_CH;
    const int r = lane & 15, g = lane >> 4;
    const int e = (-2 * r) & 7;
    const int kb = (e == 0) ? KB0 : (e == 2 ? KB2 : (e == 4 ? KB4 : KB6));
    const int ub = (r & 1) ? UB1 : UB0;
    const int ubv = (int)cb + ub;
    const int ulv = ubv + 16 * (18 * (r >> 1) + g);
    int c0 = v4 == 0 ? -1952 : (v4 == 1 ? -1056 : (v4 == 2 ? -928 : -288));
    unsigned aad = cb + (unsigned)(kb + 16 * (g - ((r + 3) >> 2)) + 2 * KZ_ + 2 * c0 - 192);
    auto boff = [&](int c) { const int c8 = c >> 3; return 16 * (c8 + 2 * (c8 >> 4) + 4); };
#define CV_B(dst, c, T) do { int a_ = ulv + boff(c) + 2304 * (T); a_ = a_ < ubv ? ubv : a_; DS_B128(dst.u, a_, 0); } while (0)
#define MF16(acc_, A_, B_) asm volatile("v_mfma_f32_16x16x32_f16 %0, %1, %2, %0" : "+v"(acc_) : "v"(A_.u), "v"(B_.u))
    f32x4 a0[4] = {}, a1[4] = {};
    if (v4 < 3) {
      const int T = (v4 == 2) ? 0 : 1;
      const int nit = v4 == 0 ? 7 : (v4 == 1 ? 6 : 5);
      Frag W0, W1, W2, W3, B0, B1, B2, B3;
      asm volatile("" : "+v"(a1[0]), "+v"(a1[1]), "+v"(a1[2]), "+v"(a1[3]));
      DS_B128(W1.u, aad, 0); DS_B128(W2.u, aad, 64); DS_B128(W3.u, aad, 128);
      CV_B(B0, c0, T); DS_B128(W0.u, aad, 192); CV_B(B1, c0 + 32, T);
      SCHED();
#define CV_STEP(F3, F2, F1, F0, BC, BN, k) \
      WAIT_LGKM(2); SCHED(); MF16(a1[3], F3, BC); DS_B128(F3.u, aad, 256 + 64 * (k)); CV_B(BN, c0 + 32 * (k) + 64, T); SCHED(); \
      MF16(a1[2], F2, BC); MF16(a1[1], F1, BC); WAIT_LGKM(3); SCHED(); MF16(a1[0], F0, BC); SCHED();
#pragma unroll 1
      for (int it = 0; it < nit; ++it) {
        CV_STEP(W1, W2, W3, W0, B0, B2, 0)
        CV_STEP(W2, W3, W0, W1, B1, B3, 1)
        CV_STEP(W3, W0, W1, W2, B2, B0, 2)
        CV_STEP(W0, W1, W2, W3, B3, B1, 3)
        c0 += 128; aad += 256;
        if (it == 1 && has_tile) {
          WAIT_VM(0);
          if (lane == 0) set_flag(hflag, ep);
        }
      }
      WAIT_LGKM(0);
      MFMA_DRAIN();
      SCHED();
    } else {
      Frag W0, W1, W2, W3, B0, B1, B2, B3;
      asm volatile("" : "+v"(a1[0]), "+v"(a1[1]), "+v"(a1[2]), "+v"(a1[3]), "+v"(a0[0]), "+v"(a0[1]), "+v"(a0[2]), "+v"(a0[3]));
      DS_B128(W1.u, aad, 0); DS_B128(W2.u, aad, 64); DS_B128(W3.u, aad, 128);
      CV_B(B0, c0, 0); CV_B(B2, c0, 1); DS_B128(W0.u, aad, 192);
      SCHED();
#define CV_STEP2(F3, F2, F1, F0, BC, CC, BN, CN, k) \
      WAIT_LGKM(1); SCHED(); MF16(a0[3], F3, BC); MF16(a1[3], F3, CC); \
      CV_B(BN, c0 + 32 * (k) + 32, 0); CV_B(CN, c0 + 32 * (k) + 32, 1); DS_B128(F3.u, aad, 256 + 64 * (k)); SCHED(); \
      MF16(a0[2], F2, BC); MF16(a1[2], F2, CC); MF16(a0[1], F1, BC); MF16(a1[1], F1, CC); \
      WAIT_LGKM(3); SCHED(); MF16(a0[0], F0, BC); MF16(a1[0], F0, CC); SCHED();
#pragma unroll 1
      for (int it = 0; it < 3; ++it) {
        CV_STEP2(W1, W2, W3, W0, B0, B2, B1, B3, 0)
        CV_STEP2(W2, W3, W0, W1, B1, B3, B0, B2, 1)
        CV_STEP2(W3, W0, W1, W2, B0, B2, B1, B3, 2)
        CV_STEP2(W0, W1, W2, W3, B1, B3, B0, B2, 3)
        c0 += 128; aad += 256;
        if (it == 0 && has_tile) {
          WAIT_VM(0);
          if (lane == 0) set_flag(hflag, ep);
        }
      }
      WAIT_LGKM(0); SCHED();
      MF16(a0[3], W1, B0); MF16(a1[3], W1, B2);
      MFMA_DRAIN();
      SCHED();
    }
    RT2(4);
    LAS float* xch = (LAS float*)(smem + CV_XCH + ch * 12288);
    auto put = [&](int slot, const f32x4& p0, const f32x4& p1) {
      *(LAS f32x4*)(xch + slot * 512 + lane * 4) = p0; *(LAS f32x4*)(xch + slot * 512 + 256 + lane * 4) = p1; };
    if (v4 == 0) put(0, a1[2], a1[3]);
    else if (v4 == 1) put(1, a1[0], a1[1]);
    else if (v4 == 2) put(4, a1[2], a1[3]);
    else { put(2, a1[0], a1[1]); put(3, a1[2], a1[3]); put(5, a0[0], a0[1]); }
    BARRIER();
    RT2(5);
    f32x4 y0, y1;
    {
      const int s1 = v4 == 0 ? 1 : (v4 == 1 ? 0 : (v4 == 2 ? 5 : 4));
      y0 = v4 == 3 ? a0[2] : ((v4 & 1) ? a1[2] : a1[0]);
      y1 = v4 == 3 ? a0[3] : ((v4 & 1) ? a1[3] : a1[1]);
      y0 += *(LAS f32x4*)(xch + s1 * 512 + lane * 4); y1 += *(LAS f32x4*)(xch + s1 * 512 + 256 + lane * 4);
      if (v4 < 2) { y0 += *(LAS f32x4*)(xch + (2 + v4) * 512 + lane * 4); y1 += *(LAS f32x4*)(xch + (2 + v4) * 512 + 256 + lane * 4); }
    }
    const float ki = *(LAS float*)cbase;
    const float dk = bf2f(A.dsk[d]);
    const int Tf = v4 < 2 ? 1 : 0, t0 = 2 * (v4 & 1);
    const int l1 = 8 * Tf + (r >> 1), sft = r & 1;
    const __amdgpu_buffer_rsrc_t rs = __builtin_amdgcn_make_buffer_rsrc(A.sT, 0, D_ * L_ * 2, 0x00020000);
#pragma unroll
    for (int tt = 0; tt < 2; ++tt) {
      const f32x4 y = tt ? y1 : y0;
      const float send0 = sft ? y[0] : y[2], send1 = sft ? y[1] : y[3];
      const float rcv0 = __builtin_bit_cast(float, __builtin_amdgcn_update_dpp(0, __builtin_bit_cast(int, send0), 0xB1, 0xf, 0xf, true));
      const float rcv1 = __builtin_bit_cast(float, __builtin_amdgcn_update_dpp(0, __builtin_bit_cast(int, send1), 0xB1, 0xf, 0xf, true));
      float z[4];
      z[0] = sft ? rcv0 : y[0]; z[1] = sft ? y[2] : rcv0; z[2] = sft ? rcv1 : y[1]; z[3] = sft ? y[3] : rcv1;
      const int l = 128 * l1 + 32 * (t0 + tt) + 8 * g + 4 * sft;
      const int c = l >> 3;
      const u32x2 uu = *(LAS u32x2*)(cbase + UB0 + 16 * (c + 2 * (c >> 4) + 4) + 8 * sft);
      float o[4];
#pragma unroll
      for (int i = 0; i < 4; ++i) {
        const unsigned hw = (i & 1) ? (uu[i >> 1] >> 16) : (uu[i >> 1] & 0xffffu);
        const float uf = (float)__builtin_bit_cast(_Float16, (u16)hw);
        o[i] = bfr(z[i] * ki) + bfr(uf * dk);
      }
      __builtin_amdgcn_raw_buffer_store_b64((u32x2){pkbf(o[0], o[1]), pkbf(o[2], o[3])}, rs, (unsigned)(d * L_ + l) * 2u, 0, 16);
    }
    RT2(6);
    BARRIER();
  }
  STAMPAT(4); RT2(7);

  constexpr int NST = F_ / 64;
  u64* sflag = A.fl + FL_S + b * 8 + w;
  if (w >= 4) {
    const int j = w - 4;
    const char* abase = (const char*)(A.h + (size_t)g2m0 * HP_);
    unsigned aoff[2];
#pragma unroll
    for (int i = 0; i < 2; ++i) {
      const int r = 8 * (2 * j + i) + (lane >> 3);
      aoff[i] = (unsigned)(r * HP_ + 8 * ((lane & 7) ^ ((r >> 1) & 7))) * 2u;
    }
    const char* ap = abase;
    unsigned slot = 0;
#define G2_LDA() do { glds16_sc1(ap + aoff[0], smem + slot + (2 * j) * 1024); glds16_sc1(ap + aoff[1], smem + slot + (2 * j + 1) * 1024); } while (0)
#pragma unroll 1
    for (int s = 0; s < G2_NA - 1; ++s) { G2_LDA(); ap += 128; slot += 8192; }
    WAIT_VMN(2 * (G2_NA - 2));
    if (lane == 0) set_flag(sflag, ep);
    BARRIER();
#pragma unroll 1
    for (int s = 0; s <= NST - G2_NA; ++s) {
      WAIT_VMN(2 * (G2_NA - 3));
      BARRIER();
      G2_LDA();
      ap += 128;
      slot = (slot == (G2_NA - 1) * 8192) ? 0u : slot + 8192;
    }
    WAIT_VM(10); BARRIER();
    WAIT_VM(8); BARRIER();
    WAIT_VM(6); BARRIER();
    WAIT_VM(4); BARRIER();
    WAIT_VM(2); BARRIER();
    WAIT_VM(0); BARRIER();
    return;
  }
  {
    const int wm = w >> 1, wn = w & 1;
    const int mrow = 32 * wm + (lane & 31), hh = lane >> 5;
    unsigned xoff[4];
#pragma unroll
    for (int ks = 0; ks < 4; ++ks) xoff[ks] = sbase + mrow * 128 + (((2 * ks + hh) ^ ((mrow >> 1) & 7)) << 4);
    const int g4 = (lane >> 4) & 1, q = (lane & 15) >> 2, p = lane & 3;
    const unsigned woff = sbase + G2_BR + (8 * hh + q) * 128 + (((2 * wn + (p & 1)) ^ (q & 2)) * 32) + 8 * (2 * g4 + (p >> 1));
    f32x16 acc = {};
    struct G2Frags { Frag x[4]; Frag w[4]; } FA, FB;
#define G2_RD(S, sa, sb) do { _Pragma("unroll") for (int ks = 0; ks < 4; ++ks) { \
      DS_B128(S.x[ks].u, xoff[ks] + (sa), 0); DS_TR(S.w[ks].s.a, woff + (sb), ks * 2048); DS_TR(S.w[ks].s.b, woff + (sb), ks * 2048 + 512); } } while (0)
#define G2_MM(S) do { _Pragma("unroll") for (int ks = 0; ks < 4; ++ks) MFMA32_BF16(acc, S.w[ks].u, S.x[ks].u); } while (0)
#define G2_RD1(S, ks, sa, sb) do { DS_B128(S.x[ks].u, xoff[ks] + (sa), 0); DS_TR(S.w[ks].s.a, woff + (sb), ks * 2048); DS_TR(S.w[ks].s.b, woff + (sb), ks * 2048 + 512); } while (0)
#define G2_IT(CUR, NXT) do { \
    asm volatile("" ::: "memory"); __builtin_amdgcn_s_barrier(); asm volatile("" ::: "memory"); SCHED(); \
    WAIT_LGKM(9); SCHED(); MFMA32_BF16(acc, CUR.w[0].u, CUR.x[0].u); G2_RD1(NXT, 0, an, bn); SCHED(); \
    WAIT_LGKM(9); SCHED(); MFMA32_BF16(acc, CUR.w[1].u, CUR.x[1].u); G2_RD1(NXT, 1, an, bn); SCHED(); \
    WAIT_LGKM(9); SCHED(); MFMA32_BF16(acc, CUR.w[2].u, CUR.x[2].u); G2_RD1(NXT, 2, an, bn); SCHED(); \
    WAIT_LGKM(9); SCHED(); MFMA32_BF16(acc, CUR.w[3].u, CUR.x[3].u); G2_RD1(NXT, 3, an, bn); SCHED(); \
    an = (an == (G2_NA - 1) * 8192) ? 0u : an + 8192; bn = (bn == (G2_NB - 1) * 8192) ? 0u : bn + 8192; } while (0)
    const size_t go = (size_t)(g2m0 + mrow) * D_ + g2n0 + 32 * wn + 16 * hh;
    u32x4 xa, xb;
    asm volatile("global_load_dwordx4 %0, %2, off\n\tglobal_load_dwordx4 %1, %2, off offset:16" : "=&v"(xa), "=&v"(xb) : "v"(A.x + go) : "memory");
    BARRIER(); SCHED();
    RT2(8);
    G2_RD(FA, 0u, 0u);
    WAIT_VM(2);
    if (lane == 0) set_flag(sflag, ep);
    SCHED();
    unsigned an = 8192, bn = 8192;
#pragma unroll 1
    for (int s = 0; s < 26; s += 2) { G2_IT(FA, FB); G2_IT(FB, FA); }
    G2_IT(FA, FB);
    WAIT_LGKM(0); SCHED();
    G2_MM(FB);
    MFMA_DRAIN();
    SCHED();
    STAMPAT(5); RT2(9);
    asm volatile("s_waitcnt vmcnt(0)" : "+v"(xa), "+v"(xb) :: "memory");
    BARRIER();
    float o[16];
#pragma unroll
    for (int a = 0; a < 4; ++a) {
      const s16x4 sv = trread(smem + ST_OFF + (32 * wn + 16 * hh + 4 * a + q) * 144 + (32 * wm + 16 * g4 + 4 * p) * 2);
#pragma unroll
      for (int i = 0; i < 4; ++i) {
        const int e = 4 * a + i;
        const float ov = bfr(acc[e]);
        const float gv = bfr(ov * __builtin_amdgcn_rcpf(1.0f + __builtin_amdgcn_exp2f(-1.4426950408889634f * ov)));
        const float sf = bf2f((unsigned)(u16)sv[i]);
        const unsigned xw = e < 8 ? xa[e >> 1] : xb[(e - 8) >> 1];
        const float xf = (e & 1) ? bfhi(xw) : bflo(xw);
        o[e] = xf + bfr(sf * gv);
      }
    }
    u32x4 va, vb;
#pragma unroll
    for (int j = 0; j < 4; ++j) { va[j] = pkbf(o[2 * j], o[2 * j + 1]); vb[j] = pkbf(o[8 + 2 * j], o[9 + 2 * j]); }
    *(u32x4*)(A.out + go) = va;
    *(u32x4*)(A.out + go + 8) = vb;
    STAMPAT(6);
    STAMPAT(7); STAMPRT(1, 1); RT2(10);
  }
}

#if defined(__HIP_DEVICE_COMPILE__)
#pragma clang attribute pop
#endif

extern "C" void kernel_launch(void* const* d_in, const int* in_sizes, int n_in, void* d_out, int out_size,
                              void* d_ws, size_t ws_size, hipStream_t stream) {
  char* ws = (char*)d_ws;
  KFArgs a;
  a.x = (const u16*)d_in[0]; a.lAr = (const u16*)d_in[1]; a.Aim = (const u16*)d_in[2]; a.Cr = (const u16*)d_in[3];
  a.Ci = (const u16*)d_in[4]; a.ldt = (const u16*)d_in[5]; a.dsk = (const u16*)d_in[6];
  a.wg = (const u16*)d_in[8]; a.wu = (const u16*)d_in[9]; a.wd = (const u16*)d_in[10];
  a.xn = (u16*)(ws + OFF_XN); a.uT = (u16*)(ws + OFF_UT);
  a.h = (u16*)(ws + OFF_H); a.sT = (u16*)(ws + OFF_ST); a.fl = (u64*)(ws + OFF_CNT); a.out = (u16*)d_out;
  (void)hipFuncSetAttribute((const void*)kf_main, hipFuncAttributeMaxDynamicSharedMemorySize, KF_LDS);
  kf_main<<<256, 1024, KF_LDS, stream>>>(a);
}
```

```cpp
#include <hip/hip_runtime.h>
#include <hip/hip_bf16.h>
#include <stdint.h>

#if defined(__HIP_DEVICE_COMPILE__)
#pragma clang attribute push(__attribute__((target("no-packed-fp32-ops"))), apply_to = function)
#endif

#define U2F(x) __builtin_bit_cast(float, (unsigned)(x))
#define F2U(x) __builtin_bit_cast(unsigned, (float)(x))
#define TIDX ((int)__builtin_amdgcn_workitem_id_x())
#define BIDX ((int)__builtin_amdgcn_workgroup_id_x())

typedef unsigned short u16;
typedef __attribute__((ext_vector_type(4))) short s16x4;
typedef __attribute__((ext_vector_type(8))) __bf16 bf16x8;
typedef __attribute__((ext_vector_type(8))) _Float16 f16x8;
typedef __attribute__((ext_vector_type(2))) _Float16 f16x2;
typedef __attribute__((ext_vector_type(2))) __fp16 h16x2;
typedef __attribute__((ext_vector_type(4))) float f32x4;
typedef __attribute__((ext_vector_type(2))) float f32x2;
typedef __attribute__((ext_vector_type(16))) float f32x16;
typedef __attribute__((ext_vector_type(4))) unsigned u32x4;
typedef __attribute__((ext_vector_type(2))) unsigned u32x2;
#define LAS __attribute__((address_space(3)))
#define GAS __attribute__((address_space(1)))

constexpr int L_ = 2048, D_ = 512, N_ = 128, F_ = 1792;
constexpr int HP_ = 1856;
constexpr int KZ_ = 2080;

constexpr size_t OFF_FL = 0;
constexpr size_t OFF_UT = 256u << 10;
constexpr size_t OFF_ST = OFF_UT + (2u << 20);
constexpr size_t OFF_H = OFF_ST + (2u << 20);

__device__ __forceinline__ float bf2f(unsigned v) { return U2F(v << 16); }
__device__ __forceinline__ float bflo(unsigned v) { return U2F(v << 16); }
__device__ __forceinline__ float bfhi(unsigned v) { return U2F(v & 0xffff0000u); }
__device__ __forceinline__ float bfr(float f) { return (float)(__bf16)f; }
__device__ __forceinline__ unsigned pkbf(float a, float b) {
  typedef __attribute__((ext_vector_type(2))) __bf16 bf16x2;
  bf16x2 v; v.x = (__bf16)a; v.y = (__bf16)b;
  return __builtin_bit_cast(unsigned, v);
}
__device__ __forceinline__ unsigned pkh(float a, float b) {
  f16x2 v; v.x = (_Float16)a; v.y = (_Float16)b;
  return __builtin_bit_cast(unsigned, v);
}
__device__ __forceinline__ void glds16(const void* g, LAS void* l) {
  __builtin_amdgcn_global_load_lds((const GAS void*)g, l, 16, 0, 0);
}
__device__ __forceinline__ void glds16_sc1(const void* g, LAS void* l) {
  __builtin_amdgcn_global_load_lds((const GAS void*)g, l, 16, 0, 16);
}
__device__ __forceinline__ s16x4 trread(LAS char* p) {
  return __builtin_amdgcn_ds_read_tr16_b64_v4i16((LAS s16x4*)p);
}
union Frag { struct { s16x4 a, b; } s; bf16x8 bf; f16x8 h; u32x4 u; };

#define WAIT_VM(n) asm volatile("s_waitcnt vmcnt(" #n ")" ::: "memory")
#define WAIT_VMN(n) asm volatile("s_waitcnt vmcnt(%0)" :: "i"(n) : "memory")
#define BARRIER() do { asm volatile("s_waitcnt lgkmcnt(0)" ::: "memory"); __builtin_amdgcn_s_barrier(); asm volatile("" ::: "memory"); } while (0)

#define DS_TR(dst, addr, off) asm volatile("ds_read_b64_tr_b16 %0, %1 offset:%2" : "=v"(dst) : "v"(addr), "i"(off))
#define DS_B128(dst, addr, off) asm volatile("ds_read_b128 %0, %1 offset:%2" : "=v"(dst) : "v"(addr), "i"(off))
#define WAIT_LGKM(n) asm volatile("s_waitcnt lgkmcnt(" #n ")" ::: "memory")
#define SCHED() __builtin_amdgcn_sched_barrier(0)
#define MFMA32_BF16(acc, a, b) asm volatile("v_mfma_f32_32x32x16_bf16 %0, %1, %2, %0" : "+v"(acc) : "v"(a), "v"(b))
#define MFMA_DRAIN() asm volatile("s_nop 15\n\ts_nop 7" ::: "memory")

__device__ __forceinline__ float wave_sum(float v) {
  v += __builtin_bit_cast(float, __builtin_amdgcn_update_dpp(0, __builtin_bit_cast(int, v), 0xB1, 0xf, 0xf, true));
  v += __builtin_bit_cast(float, __builtin_amdgcn_update_dpp(0, __builtin_bit_cast(int, v), 0x4E, 0xf, 0xf, true));
  v += __builtin_bit_cast(float, __builtin_amdgcn_update_dpp(0, __builtin_bit_cast(int, v), 0x141, 0xf, 0xf, true));
  v += __builtin_bit_cast(float, __builtin_amdgcn_update_dpp(0, __builtin_bit_cast(int, v), 0x140, 0xf, 0xf, true));
  const float r0 = __builtin_bit_cast(float, __builtin_amdgcn_readlane(__builtin_bit_cast(int, v), 0));
  const float r1 = __builtin_bit_cast(float, __builtin_amdgcn_readlane(__builtin_bit_cast(int, v), 16));
  const float r2 = __builtin_bit_cast(float, __builtin_amdgcn_readlane(__builtin_bit_cast(int, v), 32));
  const float r3 = __builtin_bit_cast(float, __builtin_amdgcn_readlane(__builtin_bit_cast(int, v), 48));
  return (r0 + r1) + (r2 + r3);
}
__device__ __forceinline__ void cexp_p(float arl2, float wh, float wl, float p, float& re, float& im) {
  const float mag = __builtin_amdgcn_exp2f(arl2 * p);
  const float fr = __builtin_fmaf(wl, p, __builtin_amdgcn_fractf(wh * p));
  re = mag * __builtin_amdgcn_cosf(fr);
  im = mag * __builtin_amdgcn_sinf(fr);
}
__device__ __forceinline__ void split_h(float a, float b, unsigned& hi, unsigned& lo) {
  h16x2 h = __builtin_amdgcn_cvt_pkrtz(a, b);
  float ra = a - (float)h.x, rb = b - (float)h.y;
  h16x2 l = __builtin_amdgcn_cvt_pkrtz(ra, rb);
  hi = __builtin_bit_cast(unsigned, h);
  lo = __builtin_bit_cast(unsigned, l);
}

constexpr int G1_A = 16384, G1_W = 32768, G1_STAGE = G1_A + G1_W;
constexpr int KF_LDS = 163840;
constexpr int HS_PART = 98304;
constexpr int HS_CS = 147456;
constexpr int HS_ST = HS_CS + 1024;
constexpr int HS_PRM = 158720;
constexpr int CV_KS = 4672, CV_US = 4736, CV_CH = 4 * CV_KS + 2 * CV_US;
constexpr int KB2 = 64, KB4 = 64 + CV_KS, KB6 = 64 + 2 * CV_KS, KB0 = 64 + 3 * CV_KS - 16;
constexpr int UB0 = 4 * CV_KS + 64, UB1 = UB0 + CV_US;
constexpr int CV_XCH = 57344;
constexpr int G2_BR = 81920, G2_NB = 10, G2_NA = 10;
constexpr int ST_OFF = 2 * 8192;
constexpr int FL_LN = 0, FL_H = 2048, FL_S = 4096, FL_U = 6144, FL_CS = 8192;

__device__ __forceinline__ float gelu_t(float x) {
  const float c1 = -2.0f * 0.7978845608028654f * 1.4426950408889634f;
  const float c2 = c1 * 0.044715f;
  float a = x * __builtin_fmaf(x * x, c2, c1);
  float e = __builtin_amdgcn_exp2f(a);
  return x * __builtin_amdgcn_rcpf(1.0f + e);
}
typedef unsigned long long u64;
extern "C" __device__ u64 kf_dispatch_id() __asm("llvm.amdgcn.dispatch.id");
__device__ __forceinline__ u64 launch_epoch() {
  const u64 x = kf_dispatch_id() * 0x9E3779B97F4A7C15ull + (u64)(size_t)__builtin_amdgcn_dispatch_ptr() * 0xC2B2AE3D27D4EB4Full;
  return (x ^ (x >> 29)) | 0x8000000000000001ull;
}
__device__ __forceinline__ void set_flag(u64* p, u64 e) { __hip_atomic_store((GAS u64*)p, e, __ATOMIC_RELAXED, __HIP_MEMORY_SCOPE_AGENT); }
__device__ __forceinline__ void put_gran(u64* p, unsigned v, u64 e) { set_flag(p, (e & 0xffffffff00000000ull) | v); }
__device__ __forceinline__ unsigned get_gran(const u64* p, u64 g, u64 e) {
  for (unsigned spins = 0; spins < (1u << 20) && __builtin_amdgcn_ballot_w64((g >> 32) != (e >> 32)) != 0ull; ++spins) {
    __builtin_amdgcn_s_sleep(1);
    g = __hip_atomic_load((GAS u64*)p, __ATOMIC_RELAXED, __HIP_MEMORY_SCOPE_AGENT);
  }
  return (unsigned)g;
}
__device__ __forceinline__ void wait_flags2(const u64* p0, const u64* p1, u64 e) {
  for (unsigned spins = 0; spins < (1u << 20); ++spins) {
    const u64 a = __hip_atomic_load((GAS u64*)p0, __ATOMIC_RELAXED, __HIP_MEMORY_SCOPE_AGENT);
    const u64 b = __hip_atomic_load((GAS u64*)p1, __ATOMIC_RELAXED, __HIP_MEMORY_SCOPE_AGENT);
    if (__builtin_amdgcn_ballot_w64(a != e || b != e) == 0ull) break;
    __builtin_amdgcn_s_sleep(1);
  }
}

struct KIdx { u16 v[2304]; };
constexpr KIdx make_kidx() {
  KIdx t{};
  for (int x = 0; x < 2304; ++x) {
    const int idx = KZ_ - x;
    int o = 21 * 32 + 1;
    if (idx >= 0 && idx < L_) {
      int p = idx;
      int sh = idx >= 1024 ? 3 : (idx >= 512 ? 2 : (idx >= 256 ? 1 : 0));
      if (sh) {
        const int q = idx >> sh, rem = idx & ((1 << sh) - 1), half = 1 << (sh - 1);
        p = (q + ((rem > half || (rem == half && (q & 1))) ? 1 : 0)) << sh;
      }
      const int r = p & 7, cc = p >> 3, c1 = cc >> 5, c0 = cc & 31;
      const int rw = c1 == 0 ? r : (c1 == 1 ? 8 + (r >> 1) : (c1 < 4 ? 8 + 2 * c1 + (r >> 2) : 12 + c1));
      o = rw * 32 + c0;
    }
    t.v[x] = (u16)(o * 4);
  }
  return t;
}
__device__ const KIdx g_kidx = make_kidx();

struct G1Frags { Frag x[2]; Frag w[2]; };
struct KFArgs {
  const u16 *x, *lAr, *Aim, *Cr, *Ci, *ldt, *dsk, *wg, *wu, *wd;
  u16 *uT, *h, *sT; u64* fl; u16* out;
};

__device__ __forceinline__ unsigned mov32(unsigned x) { unsigned r; asm("v_mov_b32 %0, %1" : "=v"(r) : "v"(x)); return r; }
__device__ __forceinline__ void split_h2(float a, float b, unsigned& hi, unsigned& lo) {
  const h16x2 h = __builtin_amdgcn_cvt_pkrtz(a, b);
  f16x2 l;
  l.x = (_Float16)__builtin_fmaf((float)h.x, -1.0f, a);
  l.y = (_Float16)__builtin_fmaf((float)h.y, -1.0f, b);
  hi = __builtin_bit_cast(unsigned, h);
  lo = __builtin_bit_cast(unsigned, l);
}
#define HBAR() do { SCHED(); asm volatile("" ::: "memory"); __builtin_amdgcn_s_barrier(); asm volatile("" ::: "memory"); SCHED(); } while (0)
__device__ __forceinline__ void kf_helper(LAS char* smem, const KFArgs& A, int j, int lane, int g2m0, int g2n0, u64 ep, bool has_tile, int tmt, int tn0, int tkb) {
  const int b = BIDX, ch = j >> 2, nq = j & 3, d = 2 * b + ch;
  LAS float* prm = (LAS float*)(smem + HS_PRM) + j * 160;
  LAS float* part = (LAS float*)(smem + HS_PART) + (j >> 1) * 704;
  const int uc = 64 * nq + lane;
  unsigned warm = 0, warm2 = 0;
  const int ht = j * 64 + lane, bi = b >> 3;
#define TOUCH(reg, ptr) asm volatile("global_load_dword %0, %1, off" : "+v"(reg) : "v"(ptr) : "memory")
  const u32x4 kx0 = *(const u32x4*)(g_kidx.v + 8 * (4 + uc));
  const int c2 = lane < 4 ? lane : 256 + (lane < 21 ? lane : 20);
  const u32x4 kx1 = *(const u32x4*)(g_kidx.v + 8 * c2);
  float kinv;
  u32x2 xt[4] = {};
  {
    const int n = d * N_ + nq * 32 + (lane & 31);
    const u32x4 xv = *(const u32x4*)(A.x + (size_t)(b * 8 + j) * D_ + lane * 8);
    const unsigned vdt = A.ldt[d];
    const unsigned a = *(const unsigned*)(A.Cr + d * N_ + 2 * lane), bb = *(const unsigned*)(A.Ci + d * N_ + 2 * lane);
    const unsigned v0 = A.lAr[n], v1 = A.Aim[n], v2 = A.Cr[n], v3 = A.Ci[n];
    HBAR();
    if (tkb >= 0) {
      const int q = (lane & 15) >> 2, p = lane & 3, chk = 2 * (j & 3) + (p >> 1);
#pragma unroll
      for (int t = 0; t < 4; ++t) {
        const int ra = 64 * (j >> 2) + 32 * (t >> 1) + 8 * (lane >> 4) + 4 * (t & 1) + q;
        xt[t] = __builtin_bit_cast(u32x2, trread(smem + ra * 128 + ((chk ^ ((ra >> 1) & 7)) << 4) + 8 * (p & 1)));
      }
    }
    {
      float f[8];
#pragma unroll
      for (int e = 0; e < 4; ++e) { f[2 * e] = bflo(xv[e]); f[2 * e + 1] = bfhi(xv[e]); }
      const float mean = wave_sum(((f[0] + f[1]) + (f[2] + f[3])) + ((f[4] + f[5]) + (f[6] + f[7]))) * (1.0f / D_);
      float q2 = 0.f;
#pragma unroll
      for (int e = 0; e < 8; ++e) { const float dd = f[e] - mean; q2 = __builtin_fmaf(dd, dd, q2); }
      const float var = wave_sum(q2) * (1.0f / D_);
      const float sd = __builtin_sqrtf(bfr(bfr(var) + 1e-5f));
      if (lane == 0) put_gran(A.fl + FL_LN + b * 8 + j, pkbf(mean, sd), ep);
    }
    const float dt = bfr(__builtin_expf(bf2f(vdt)));
    const float sb = wave_sum((__builtin_fabsf(bflo(a)) + __builtin_fabsf(bfhi(a))) + (__builtin_fabsf(bflo(bb)) + __builtin_fabsf(bfhi(bb)))) * dt * 2.0f;
    const int ex = (int)((F2U(sb) >> 23) & 0xff) - 127;
    int se = 14 - ex;
    se = se > 100 ? 100 : (se < -100 ? -100 : se);
    const float cs = dt * 2.0f * U2F((unsigned)(127 + se) << 23);
    kinv = U2F((unsigned)(127 - se) << 23);
    const float ar = -bfr(__builtin_expf(bf2f(v0))) * dt;
    const float ai = bf2f(v1) * dt;
    const float w0 = ai * 0.15915494309189535f;
    const float wh = U2F(F2U(w0) & 0xfffff000u);
    const float wl = __builtin_fmaf(ai, 6.4206383e-09f, __builtin_fmaf(ai, 0.15915494309189535f, -wh));
    if (lane < 32) {
      prm[lane] = ar * 1.4426950408889634f; prm[32 + lane] = wh; prm[64 + lane] = wl;
      prm[96 + lane] = bf2f(v2) * cs; prm[128 + lane] = bf2f(v3) * cs;
    }
  }
  const int row = lane & 31, hh = lane >> 5;
  int prow;
  if (row < 8) prow = row;
  else if (row < 12) prow = 256 + 2 * (row - 8);
  else if (row < 16) prow = 512 + 256 * ((row - 12) >> 1) + 4 * ((row - 12) & 1);
  else prow = 1024 + 256 * (row - 16);
  prow = row < 21 ? prow : 0;
  const float pA = (float)prow, pB = (float)(8 * row);
  f32x16 acc = {};
  const int pnl = 4 * nq + ((lane & 7) < 4 ? (lane & 7) : 4);
  const u64* f0 = A.fl + FL_U + (pnl < 16 ? pnl : 15) * 64 + (d >> 6) * 8 + ((d >> 4) & 3) + 4 * ((lane >> 3) & 1);
  const u64* gp = j < 2 ? A.fl + FL_LN + tmt * 128 + ht : A.fl + FL_CS + ((j >> 1) & 1) * F_ + tn0 + (j & 1) * 64 + lane;
  u64 fa = 0, gr = 0;
  typedef __attribute__((ext_vector_type(4))) _Float16 f16x4;
#pragma unroll 1
  for (int hs = 0; hs < 8; ++hs) {
    if (hs == 3 || hs == 5 || hs == 7) {
      asm volatile("s_waitcnt vmcnt(0)" : "+v"(gr) :: "memory");
      if (hs == 3 && has_tile && j < 2) {
        const unsigned g = get_gran(gp, gr, ep);
        *(LAS f32x2*)(smem + HS_ST + ht * 8) = (f32x2){bflo(g), __builtin_amdgcn_rcpf(bfhi(g))};
      }
      if (hs == 5 && tkb >= 0 && lane == 0) set_flag(A.fl + FL_U + (tmt * 8 + tkb) * 8 + j, ep);
      if (hs == 7) {
        if (has_tile && j >= 4) *(LAS float*)(smem + HS_CS + (ht - 256) * 4) = U2F(get_gran(gp, gr, ep));
        asm volatile("global_load_dwordx2 %0, %1, off sc1" : "+v"(fa) : "v"(f0) : "memory");
      }
    }

    BARRIER();
    if (has_tile && ((hs == 1 && j < 2) || (hs == 5 && j >= 4)))
      asm volatile("global_load_dwordx2 %0, %1, off sc1" : "+v"(gr) : "v"(gp) : "memory");
    if (hs == 3 && tkb >= 0) {
      const __amdgpu_buffer_rsrc_t ru = __builtin_amdgcn_make_buffer_rsrc(A.uT, 0, L_ * D_ * 2, 0x00020000);
#pragma unroll
      for (int t = 0; t < 2; ++t) {
        const int r0 = 64 * (j >> 2) + 32 * t + 8 * (lane >> 4);
        u32x4 o;
#pragma unroll
        for (int e = 0; e < 4; ++e) {
          const f32x4 st = *(const LAS f32x4*)(smem + HS_ST + (r0 + 2 * e) * 8);
          const unsigned xw = xt[2 * t + (e >> 1)][e & 1];
          const unsigned tt = pkbf(bflo(xw) - st[0], bfhi(xw) - st[2]);
          const unsigned oo = pkbf(bflo(tt) * st[1], bfhi(tt) * st[3]);
          o[e] = __builtin_bit_cast(unsigned, __builtin_amdgcn_cvt_pkrtz(bflo(oo), bfhi(oo)));
        }
        __builtin_amdgcn_raw_buffer_store_b128(o, ru, (unsigned)((64 * tkb + 16 * (j & 3) + (lane & 15)) * L_ + 128 * tmt + r0) * 2u, 0, 16);
      }
    }
    const int n0 = hs * 4 + hh * 2;
    const f32x2 va = *(LAS f32x2*)(prm + n0), vh = *(LAS f32x2*)(prm + 32 + n0), vl = *(LAS f32x2*)(prm + 64 + n0);
    const f32x2 vr = *(LAS f32x2*)(prm + 96 + n0), vi = *(LAS f32x2*)(prm + 128 + n0);
    u32x2 Ah, Al, Bh, Bl;
#pragma unroll
    for (int q = 0; q < 2; ++q) {
      float er, ei, yr, yi;
      cexp_p(va[q], vh[q], vl[q], pA, er, ei);
      cexp_p(va[q], vh[q], vl[q], pB, yr, yi);
      const float xr = vr[q] * er - vi[q] * ei, xi = vr[q] * ei + vi[q] * er;
      unsigned h0, l0, h1, l1;
      split_h(xr, xi, h0, l0);
      split_h(yr, -yi, h1, l1);
      Ah[q] = h0; Al[q] = l0; Bh[q] = h1; Bl[q] = l1;
    }
    acc = __builtin_amdgcn_mfma_f32_32x32x8f16(__builtin_bit_cast(f16x4, Al), __builtin_bit_cast(f16x4, Bh), acc, 0, 0, 0);
    acc = __builtin_amdgcn_mfma_f32_32x32x8f16(__builtin_bit_cast(f16x4, Ah), __builtin_bit_cast(f16x4, Bl), acc, 0, 0, 0);
    acc = __builtin_amdgcn_mfma_f32_32x32x8f16(__builtin_bit_cast(f16x4, Ah), __builtin_bit_cast(f16x4, Bh), acc, 0, 0, 0);
  }
  if ((j & 1) == 0) {
#pragma unroll
    for (int i = 0; i < 12; ++i) {
      const int rr = (i & 3) + 8 * (i >> 2) + 4 * hh;
      if (rr < 21) part[rr * 32 + row] = acc[i];
    }
    if (lane == 0) part[21 * 32 + 1] = 0.f;
  }
  BARRIER();
  asm volatile("s_waitcnt vmcnt(0)" : "+v"(fa) :: "memory");
  if (__builtin_amdgcn_ballot_w64(fa != ep) != 0ull) wait_flags2(f0, f0, ep);
  u32x4 ua = {0u, 0u, 0u, 0u}; unsigned unx = 0;
  {
    const u16* ur = A.uT + (size_t)d * L_ + 8 * uc;
    asm volatile("global_load_dwordx4 %0, %2, off sc1\n\tglobal_load_ushort %1, %3, off sc1" : "+v"(ua), "+v"(unx) : "v"(ur), "v"(ur + (uc < 255 ? 8 : 0)) : "memory");
  }
  if (j & 1) {
#pragma unroll
    for (int i = 0; i < 12; ++i) {
      const int rr = (i & 3) + 8 * (i >> 2) + 4 * hh;
      if (rr < 21) part[rr * 32 + row] += acc[i];
    }
  }
  BARRIER();
  {
    const LAS char* t0 = smem + HS_PART + (2 * ch) * 2816;
    LAS char* cbase = smem + ch * CV_CH;
    auto expand = [&](const u32x4& kx, int c) {
      float v[8];
#pragma unroll
      for (int e = 0; e < 8; ++e) {
        const unsigned o = (e & 1) ? (kx[e >> 1] >> 16) : (kx[e >> 1] & 0xffffu);
        v[e] = *(const LAS float*)(t0 + o) + *(const LAS float*)(t0 + o + 2816);
      }
      u32x4 o;
#pragma unroll
      for (int e = 0; e < 4; ++e) o[e] = pkh(v[2 * e], v[2 * e + 1]);
      *(LAS u32x4*)(cbase + KB0 + 16 * c) = o;
      LAS unsigned* q2 = (LAS unsigned*)(cbase + KB2 + 16 * c - 4);
      LAS u32x2* q4 = (LAS u32x2*)(cbase + KB4 + 16 * c - 8);
      LAS unsigned* q6 = (LAS unsigned*)(cbase + KB6 + 16 * c - 12);
#pragma unroll
      for (int e = 0; e < 4; ++e) { q2[e] = o[e]; q6[e] = o[e]; }
      q4[0] = (u32x2){o[0], o[1]}; q4[1] = (u32x2){o[2], o[3]};
    };
    expand(kx0, 4 + uc);
    if (nq == 0) {
      if (lane < 21) expand(kx1, c2);
      if (lane == 0) *(LAS float*)(cbase) = kinv;
    }
    asm volatile("s_waitcnt vmcnt(0)" : "+v"(ua), "+v"(unx) :: "memory");
    const unsigned nx = (uc < 255) ? unx : 0u;
    const int P = uc + 2 * (uc >> 4) + 4;
    *(LAS u32x4*)(cbase + UB0 + 16 * P) = ua;
    u32x4 s1;
    s1[0] = (ua[0] >> 16) | (ua[1] << 16); s1[1] = (ua[1] >> 16) | (ua[2] << 16);
    s1[2] = (ua[2] >> 16) | (ua[3] << 16); s1[3] = (ua[3] >> 16) | (nx << 16);
    *(LAS u32x4*)(cbase + UB1 + 16 * P) = s1;
    if (uc == 0) {
      const u32x4 z = {0u, 0u, 0u, 0u};
      *(LAS u32x4*)(cbase + UB0) = z; *(LAS u32x4*)(cbase + UB0 + 16) = z;
      *(LAS u32x4*)(cbase + UB1) = z; *(LAS u32x4*)(cbase + UB1 + 16) = (u32x4){0u, 0u, 0u, ua[0] << 16};
    }
  }

  BARRIER();

  {
    constexpr int NST = F_ / 64;
    const char* bbase = (const char*)(A.wd + g2n0);
    const int r = 8 * j + (lane >> 3);
    const unsigned boff = (unsigned)(r * D_) * 2u + (unsigned)((((lane & 7) >> 1) ^ (r & 2)) * 32 + (lane & 1) * 16);
    const char* bp = bbase + boff;
    unsigned slot = 0;
    TOUCH(warm, (const char*)A.wd + (size_t)(bi * 448 + (ht < 448 ? ht : 447)) * 128);
#pragma unroll 1
    for (int s = 0; s < 8; ++s) { glds16(bp, smem + G2_BR + slot + j * 1024); bp += 128 * D_; slot += 8192; }
    BARRIER();
    if (j == 0) { const u64* hf = A.fl + FL_H + (g2m0 >> 7) * 128; wait_flags2(hf + lane, hf + (lane < 48 ? 64 + lane : lane), ep); }
    BARRIER();
    {
      const int t = (g2n0 >> 6) * 224 + (ht < 224 ? ht : 223);
      const int ln = t >> 6, r = t & 63;
      TOUCH(warm2, (const char*)A.h + (size_t)(g2m0 + r) * (HP_ * 2) + ln * 128);
    }
    asm volatile("s_waitcnt vmcnt(8)" : "+v"(warm) :: "memory");
    BARRIER();
    const u64* sf = A.fl + FL_S + (g2n0 >> 6) * 256 + 32 * j + (lane & 31);
    u64 sflag = 0;
    u32x4 sv = {0u, 0u, 0u, 0u};
    const int sc = j * 64 + lane;
#pragma unroll 1
    for (int i = 0; i < 10; ++i) {
      WAIT_VM(5);
      BARRIER();
      if (i == 6) asm volatile("global_load_dwordx2 %0, %1, off sc1" : "+v"(sflag) : "v"(sf) : "memory");
      glds16(bp, smem + G2_BR + slot + j * 1024);
      glds16(bp + 128 * D_, smem + G2_BR + slot + 8192 + j * 1024);
      bp += 256 * D_;
      slot = (slot == 8 * 8192) ? 0u : slot + 16384;
    }
    asm volatile("s_waitcnt vmcnt(5)" : "+v"(sflag) :: "memory");
    BARRIER();
    {
      if (__builtin_amdgcn_ballot_w64(sflag != ep) != 0ull) wait_flags2(sf, sf, ep);
      const __amdgpu_buffer_rsrc_t rs = __builtin_amdgcn_make_buffer_rsrc(A.sT, 0, D_ * L_ * 2, 0x00020000);
      const unsigned so = (unsigned)(((g2n0 + (sc >> 3)) * L_ + g2m0 + 8 * (sc & 7)) * 2);
      asm volatile("buffer_load_dwordx4 %0, %1, %2, 0 offen sc1" : "+v"(sv) : "v"(so), "s"(rs) : "memory");
    }
    WAIT_VM(4); BARRIER();
    WAIT_VM(2); BARRIER();
    WAIT_VM(1); BARRIER();
    asm volatile("s_waitcnt vmcnt(0)" : "+v"(sv), "+v"(warm2) :: "memory");
    *(LAS u32x4*)(smem + ST_OFF + (sc >> 3) * 144 + (sc & 7) * 16) = sv;
    BARRIER();
  }
}

__global__ __launch_bounds__(1024, 4) void kf_main(KFArgs A) {
  extern __shared__ __attribute__((aligned(16))) char smem_[];
  LAS char* smem = (LAS char*)smem_;
  const unsigned sbase = (unsigned)(size_t)smem;
  const int tid = TIDX, lane = tid & 63;
  const int w = __builtin_amdgcn_readfirstlane(tid >> 6);
  const int b = BIDX, xcd = b & 7, idx = b >> 3;
  const bool has_tile = idx < 28;
  const int g2mt = xcd * 4 + (idx & 3), g2nt = idx >> 2;
  const int g2m0 = g2mt * 64, g2n0 = g2nt * 64;

  const u64 ep = launch_epoch();
  const int mt = (xcd & 3) * 4 + (idx & 3), nt = (xcd >> 2) * 7 + (has_tile ? idx >> 2 : 6);
  const int rot = nt & 7;
  if (w >= 8) { kf_helper(smem, A, w - 8, lane, g2m0, g2n0, ep, has_tile, mt, nt * 128, (has_tile && nt < 8) ? (nt & 7) : -1); return; }

  const int m0 = mt * 128, n0 = nt * 128;
  u64* hflag = A.fl + FL_H + mt * 128 + nt * 8 + w;
  const char* abase = (const char*)(A.x + (size_t)m0 * D_);
  const char* gbase = (const char*)(A.wg + n0);
  const char* ubase = (const char*)(A.wu + n0);
  const int arow = 16 * w + (lane >> 3);
  const unsigned aoff0 = (unsigned)(arow * D_ + 8 * ((lane & 7) ^ ((arow >> 1) & 7))) * 2u;
  const unsigned aoff1 = (unsigned)((arow + 8) * D_ + 8 * ((lane & 7) ^ (((arow + 8) >> 1) & 7))) * 2u;
  const int wk = 8 * w + (lane >> 3);
  const int fk = (wk >> 1) & 1;
  const unsigned woffg = (unsigned)(wk * F_) * 2u + (unsigned)((((lane & 7) >> 1) ^ fk) * 32 + (lane & 1) * 16);
  const unsigned woffu = (unsigned)(wk * F_) * 2u + (unsigned)((((lane & 7) >> 1) ^ (fk | 2)) * 32 + (lane & 1) * 16);
#define G1_LD(i, s_, sb) do { const int s = ((s_) + rot) & 7; \
    if ((i) == 0) glds16(abase + (s) * 128 + aoff0, smem + (sb) + (2 * w) * 1024); \
    if ((i) == 1) glds16(gbase + (long)(s) * (128 * F_) + woffg, smem + (sb) + G1_A + w * 1024); \
    if ((i) == 2) glds16(gbase + (long)(s) * (128 * F_) + 128 + woffg, smem + (sb) + G1_A + 8192 + w * 1024); \
    if ((i) == 3) glds16(abase + (s) * 128 + aoff1, smem + (sb) + (2 * w + 1) * 1024); \
    if ((i) == 4) glds16(ubase + (long)(s) * (128 * F_) + woffu, smem + (sb) + G1_A + 16384 + w * 1024); \
    if ((i) == 5) glds16(ubase + (long)(s) * (128 * F_) + 128 + woffu, smem + (sb) + G1_A + 24576 + w * 1024); } while (0)
  if (has_tile) {
#pragma unroll
    for (int i = 0; i < 6; ++i) G1_LD(i, 0, 0);
#pragma unroll
    for (int i = 0; i < 6; ++i) G1_LD(i, 1, G1_STAGE);
    const int wm = w & 1, wn = w >> 1;
    const int mrow = 64 * wm + (lane & 31), hh = lane >> 5;
    unsigned xoff[4];
#pragma unroll
    for (int ks = 0; ks < 4; ++ks) xoff[ks] = sbase + mrow * 128 + (((2 * ks + hh) ^ ((mrow >> 1) & 7)) << 4);
    const int mat = (lane >> 4) & 1, q = (lane & 15) >> 2, p = lane & 3;
    const int pp = ((p & 1) << 1) | (p >> 1);
    const int fq = ((q >> 1) & 1) | (mat << 1);
    const unsigned wrow = sbase + G1_A + (mat * 2 + (wn >> 1)) * 8192 + (8 * hh + q) * 128 + pp * 8;
    const unsigned woff0 = wrow + (((2 * (wn & 1)) ^ fq) * 32);
    const unsigned woff1 = wrow + (((2 * (wn & 1) + 1) ^ fq) * 32);
    f32x16 acc[2][2];
#pragma unroll
    for (int t = 0; t < 2; ++t) { acc[t][0] = (f32x16){}; acc[t][1] = (f32x16){}; }
    G1Frags FA, FB;
#define RD_W(S, t, b0, b1, ks) do { DS_TR(S.w[t].s.a, (t) ? b1 : b0, (ks) * 2048); DS_TR(S.w[t].s.b, (t) ? b1 : b0, (ks) * 2048 + 512); } while (0)
#define RD_X(S, XA) do { DS_B128(S.x[0].u, XA, 0); DS_B128(S.x[1].u, XA, 4096); } while (0)
#define MM1(S, t, m) MFMA32_BF16(acc[t][m], S.w[t].u, S.x[m].u)
    WAIT_VM(6);
    BARRIER();

    RD_X(FA, xoff[0]); RD_W(FA, 0, woff0, woff1, 0); RD_W(FA, 1, woff0, woff1, 0);
    SCHED();
    unsigned so = 0, sn = G1_STAGE, sp = 2 * G1_STAGE;
#define KSTEP(CUR, NXT, XA, KSN, L0, MORE) \
    WAIT_LGKM(2); SCHED(); MM1(CUR, 0, 0); RD_X(NXT, XA); SCHED(); \
    MM1(CUR, 0, 1); RD_W(NXT, 0, w0, w1, KSN); if (MORE) G1_LD(L0, st + 2, sp); SCHED(); \
    WAIT_LGKM(4); SCHED(); MM1(CUR, 1, 0); RD_W(NXT, 1, w0, w1, KSN); SCHED(); \
    MM1(CUR, 1, 1); if (MORE) G1_LD(L0 + 1, st + 2, sp); SCHED();
#define G1_BODY(MORE) { \
    const unsigned w0 = woff0 + so, w1 = woff1 + so; \
    const unsigned x1 = xoff[1] + so, x2 = xoff[2] + so, x3 = xoff[3] + so; \
    const unsigned xn0 = xoff[0] + sn, w0n = woff0 + sn, w1n = woff1 + sn; \
    KSTEP(FA, FB, x1, 1, 0, MORE) \
    KSTEP(FB, FA, x2, 2, 2, MORE) \
    KSTEP(FA, FB, x3, 3, 4, MORE) \
    WAIT_LGKM(2); SCHED(); MM1(FB, 0, 0); SCHED(); \
    if (MORE) WAIT_VM(6); else WAIT_VM(0); \
    BARRIER(); \
    SCHED(); \
    RD_X(FA, xn0); \
    MM1(FB, 0, 1); RD_W(FA, 0, w0n, w1n, 0); SCHED(); \
    MM1(FB, 1, 0); RD_W(FA, 1, w0n, w1n, 0); SCHED(); \
    MM1(FB, 1, 1); SCHED(); \
    sp = so; so = sn; sn = (sn == 2 * G1_STAGE) ? 0u : sn + G1_STAGE; }
#pragma unroll 1
    for (int st = 0; st < 6; ++st) G1_BODY(true)
#pragma unroll 1
    for (int st = 6; st < 8; ++st) G1_BODY(false)
    WAIT_LGKM(0);
    MFMA_DRAIN();
    SCHED();

    BARRIER();
    __builtin_amdgcn_s_setprio(2);
    const __amdgpu_buffer_rsrc_t rh = __builtin_amdgcn_make_buffer_rsrc(A.h, 0, L_ * HP_ * 2, 0x00020000);
    const unsigned ho = (unsigned)((m0 + mrow) * HP_ + n0 + 32 * wn + 8 * hh) * 2u;
#pragma unroll
    for (int t = 0; t < 2; ++t) {
      const LAS f32x4* cp = (const LAS f32x4*)(smem + HS_CS + (32 * wn + 16 * t + 8 * hh) * 4);
      const f32x4 cg0 = cp[0], cg1 = cp[1], cu0 = cp[32], cu1 = cp[33];
#pragma unroll
      for (int m = 0; m < 2; ++m) {
        const f32x2 st = *(const LAS f32x2*)(smem + HS_ST + (mrow + 32 * m) * 8);
        const float nm = -st[0] * st[1];
        u32x4 v;
#pragma unroll
        for (int j = 0; j < 4; ++j) {
          float o[2];
#pragma unroll
          for (int e = 0; e < 2; ++e) {
            const int i = 2 * j + e;
            const float g = __builtin_fmaf(acc[t][m][i], st[1], nm * (i < 4 ? cg0[i & 3] : cg1[i & 3]));
            const float u = __builtin_fmaf(acc[t][m][8 + i], st[1], nm * (i < 4 ? cu0[i & 3] : cu1[i & 3]));
            o[e] = gelu_t(g) * u;
          }
          v[j] = pkbf(o[0], o[1]);
        }
        __builtin_amdgcn_raw_buffer_store_b128(v, rh, ho + (unsigned)(m * 32 * HP_ + 16 * t) * 2u, 0, 16);
        if (t == 0 && m == 0) { SCHED(); BARRIER(); SCHED(); }
      }
    }
    __builtin_amdgcn_s_setprio(0);

    BARRIER();
  } else {
    const int q = b - 224;
    const u16* wsrc = (q < 16 ? A.wg : A.wu) + (q & 15) * 112;
    const int c = lane & 15, rs4 = lane >> 4;
    float sum[8] = {};
#pragma unroll
    for (int i = 0; i < 16; ++i) {
      const u32x4 v = *(const u32x4*)(wsrc + (size_t)(64 * w + 4 * i + rs4) * F_ + 8 * (c < 14 ? c : 13));
#pragma unroll
      for (int e = 0; e < 4; ++e) { sum[2 * e] += bflo(v[e]); sum[2 * e + 1] += bfhi(v[e]); }
    }
    LAS float* red = (LAS float*)smem;
#pragma unroll
    for (int e = 0; e < 8; ++e) red[(w * 4 + rs4) * 128 + c * 8 + e] = sum[e];
    BARRIER();
    if (tid < 112) {
      float t = 0.f;
#pragma unroll 8
      for (int i = 0; i < 32; ++i) t += red[i * 128 + tid];
      put_gran(A.fl + FL_CS + (q >> 4) * F_ + (q & 15) * 112 + tid, F2U(t), ep);
    }
#pragma unroll 1
    for (int i = 0; i < 11; ++i) BARRIER();
  }

  {
    const int ch = w >> 2, v4 = (w + 2 * ch) & 3, d = 2 * b + ch;
    LAS char* cbase = smem + ch * CV_CH;
    const unsigned cb = sbase + ch * CV_CH;
    const int r = lane & 15, g = lane >> 4;
    const int e = (-2 * r) & 7;
    const int kb = (e == 0) ? KB0 : (e == 2 ? KB2 : (e == 4 ? KB4 : KB6));
    const int ub = (r & 1) ? UB1 : UB0;
    const int ubv = (int)cb + ub;
    const int ulv = ubv + 16 * (18 * (r >> 1) + g);
    int c0 = v4 == 0 ? -1952 : (v4 == 1 ? -1056 : (v4 == 2 ? -928 : -288));
    unsigned aad = cb + (unsigned)(kb + 16 * (g - ((r + 3) >> 2)) + 2 * KZ_ + 2 * c0 - 192);
    auto boff = [&](int c) { const int c8 = c >> 3; return 16 * (c8 + 2 * (c8 >> 4) + 4); };
#define CV_B(dst, c, T) do { int a_ = ulv + boff(c) + 2304 * (T); a_ = a_ < ubv ? ubv : a_; DS_B128(dst.u, a_, 0); } while (0)
#define MF16(acc_, A_, B_) asm volatile("v_mfma_f32_16x16x32_f16 %0, %1, %2, %0" : "+v"(acc_) : "v"(A_.u), "v"(B_.u))
    f32x4 a0[4] = {}, a1[4] = {};
    if (v4 < 3) {
      const int T = (v4 == 2) ? 0 : 1;
      const int nit = v4 == 0 ? 7 : (v4 == 1 ? 6 : 5);
      Frag W0, W1, W2, W3, B0, B1, B2, B3;
      asm volatile("" : "+v"(a1[0]), "+v"(a1[1]), "+v"(a1[2]), "+v"(a1[3]));
      DS_B128(W1.u, aad, 0); DS_B128(W2.u, aad, 64); DS_B128(W3.u, aad, 128);
      CV_B(B0, c0, T); DS_B128(W0.u, aad, 192); CV_B(B1, c0 + 32, T);
      SCHED();
#define CV_STEP(F3, F2, F1, F0, BC, BN, k) \
      WAIT_LGKM(2); SCHED(); MF16(a1[3], F3, BC); DS_B128(F3.u, aad, 256 + 64 * (k)); CV_B(BN, c0 + 32 * (k) + 64, T); SCHED(); \
      MF16(a1[2], F2, BC); MF16(a1[1], F1, BC); WAIT_LGKM(3); SCHED(); MF16(a1[0], F0, BC); SCHED();
#pragma unroll 1
      for (int it = 0; it < nit; ++it) {
        CV_STEP(W1, W2, W3, W0, B0, B2, 0)
        CV_STEP(W2, W3, W0, W1, B1, B3, 1)
        CV_STEP(W3, W0, W1, W2, B2, B0, 2)
        CV_STEP(W0, W1, W2, W3, B3, B1, 3)
        c0 += 128; aad += 256;
        if (it == 1 && has_tile) {
          WAIT_VM(0);
          if (lane == 0) set_flag(hflag, ep);
        }
      }
      WAIT_LGKM(0);
      MFMA_DRAIN();
      SCHED();
    } else {
      Frag W0, W1, W2, W3, B0, B1, B2, B3;
      asm volatile("" : "+v"(a1[0]), "+v"(a1[1]), "+v"(a1[2]), "+v"(a1[3]), "+v"(a0[0]), "+v"(a0[1]), "+v"(a0[2]), "+v"(a0[3]));
      DS_B128(W1.u, aad, 0); DS_B128(W2.u, aad, 64); DS_B128(W3.u, aad, 128);
      CV_B(B0, c0, 0); CV_B(B2, c0, 1); DS_B128(W0.u, aad, 192);
      SCHED();
#define CV_STEP2(F3, F2, F1, F0, BC, CC, BN, CN, k) \
      WAIT_LGKM(1); SCHED(); MF16(a0[3], F3, BC); MF16(a1[3], F3, CC); \
      CV_B(BN, c0 + 32 * (k) + 32, 0); CV_B(CN, c0 + 32 * (k) + 32, 1); DS_B128(F3.u, aad, 256 + 64 * (k)); SCHED(); \
      MF16(a0[2], F2, BC); MF16(a1[2], F2, CC); MF16(a0[1], F1, BC); MF16(a1[1], F1, CC); \
      WAIT_LGKM(3); SCHED(); MF16(a0[0], F0, BC); MF16(a1[0], F0, CC); SCHED();
#pragma unroll 1
      for (int it = 0; it < 3; ++it) {
        CV_STEP2(W1, W2, W3, W0, B0, B2, B1, B3, 0)
        CV_STEP2(W2, W3, W0, W1, B1, B3, B0, B2, 1)
        CV_STEP2(W3, W0, W1, W2, B0, B2, B1, B3, 2)
        CV_STEP2(W0, W1, W2, W3, B1, B3, B0, B2, 3)
        c0 += 128; aad += 256;
        if (it == 0 && has_tile) {
          WAIT_VM(0);
          if (lane == 0) set_flag(hflag, ep);
        }
      }
      WAIT_LGKM(0); SCHED();
      MF16(a0[3], W1, B0); MF16(a1[3], W1, B2);
      MFMA_DRAIN();
      SCHED();
    }

    LAS float* xch = (LAS float*)(smem + CV_XCH + ch * 12288);
    auto put = [&](int slot, const f32x4& p0, const f32x4& p1) {
      *(LAS f32x4*)(xch + slot * 512 + lane * 4) = p0; *(LAS f32x4*)(xch + slot * 512 + 256 + lane * 4) = p1; };
    if (v4 == 0) put(0, a1[2], a1[3]);
    else if (v4 == 1) put(1, a1[0], a1[1]);
    else if (v4 == 2) put(4, a1[2], a1[3]);
    else { put(2, a1[0], a1[1]); put(3, a1[2], a1[3]); put(5, a0[0], a0[1]); }
    BARRIER();

    f32x4 y0, y1;
    {
      const int s1 = v4 == 0 ? 1 : (v4 == 1 ? 0 : (v4 == 2 ? 5 : 4));
      y0 = v4 == 3 ? a0[2] : ((v4 & 1) ? a1[2] : a1[0]);
      y1 = v4 == 3 ? a0[3] : ((v4 & 1) ? a1[3] : a1[1]);
      y0 += *(LAS f32x4*)(xch + s1 * 512 + lane * 4); y1 += *(LAS f32x4*)(xch + s1 * 512 + 256 + lane * 4);
      if (v4 < 2) { y0 += *(LAS f32x4*)(xch + (2 + v4) * 512 + lane * 4); y1 += *(LAS f32x4*)(xch + (2 + v4) * 512 + 256 + lane * 4); }
    }
    const float ki = *(LAS float*)cbase;
    const float dk = bf2f(A.dsk[d]);
    const int Tf = v4 < 2 ? 1 : 0, t0 = 2 * (v4 & 1);
    const int l1 = 8 * Tf + (r >> 1), sft = r & 1;
    const __amdgpu_buffer_rsrc_t rs = __builtin_amdgcn_make_buffer_rsrc(A.sT, 0, D_ * L_ * 2, 0x00020000);
#pragma unroll
    for (int tt = 0; tt < 2; ++tt) {
      const f32x4 y = tt ? y1 : y0;
      const float send0 = sft ? y[0] : y[2], send1 = sft ? y[1] : y[3];
      const float rcv0 = __builtin_bit_cast(float, __builtin_amdgcn_update_dpp(0, __builtin_bit_cast(int, send0), 0xB1, 0xf, 0xf, true));
      const float rcv1 = __builtin_bit_cast(float, __builtin_amdgcn_update_dpp(0, __builtin_bit_cast(int, send1), 0xB1, 0xf, 0xf, true));
      float z[4];
      z[0] = sft ? rcv0 : y[0]; z[1] = sft ? y[2] : rcv0; z[2] = sft ? rcv1 : y[1]; z[3] = sft ? y[3] : rcv1;
      const int l = 128 * l1 + 32 * (t0 + tt) + 8 * g + 4 * sft;
      const int c = l >> 3;
      const u32x2 uu = *(LAS u32x2*)(cbase + UB0 + 16 * (c + 2 * (c >> 4) + 4) + 8 * sft);
      float o[4];
#pragma unroll
      for (int i = 0; i < 4; ++i) {
        const unsigned hw = (i & 1) ? (uu[i >> 1] >> 16) : (uu[i >> 1] & 0xffffu);
        const float uf = (float)__builtin_bit_cast(_Float16, (u16)hw);
        o[i] = bfr(z[i] * ki) + bfr(uf * dk);
      }
      __builtin_amdgcn_raw_buffer_store_b64((u32x2){pkbf(o[0], o[1]), pkbf(o[2], o[3])}, rs, (unsigned)(d * L_ + l) * 2u, 0, 16);
    }

    BARRIER();
  }

  constexpr int NST = F_ / 64;
  u64* sflag = A.fl + FL_S + b * 8 + w;
  if (w >= 4) {
    const int j = w - 4;
    const char* abase = (const char*)(A.h + (size_t)g2m0 * HP_);
    unsigned aoff[2];
#pragma unroll
    for (int i = 0; i < 2; ++i) {
      const int r = 8 * (2 * j + i) + (lane >> 3);
      aoff[i] = (unsigned)(r * HP_ + 8 * ((lane & 7) ^ ((r >> 1) & 7))) * 2u;
    }
    const char* ap = abase;
    unsigned slot = 0;
#define G2_LDA() do { glds16_sc1(ap + aoff[0], smem + slot + (2 * j) * 1024); glds16_sc1(ap + aoff[1], smem + slot + (2 * j + 1) * 1024); \
                      ap += 128; slot = (slot == (G2_NA - 1) * 8192) ? 0u : slot + 8192; } while (0)
#pragma unroll 1
    for (int s = 0; s < 8; ++s) G2_LDA();
    WAIT_VM(16);
    if (lane == 0) set_flag(sflag, ep);
    WAIT_VM(15);
    BARRIER();
#pragma unroll 1
    for (int i = 0; i < 10; ++i) { WAIT_VM(10); BARRIER(); G2_LDA(); G2_LDA(); }
    WAIT_VM(10); BARRIER();
    WAIT_VM(6); BARRIER();
    WAIT_VM(2); BARRIER();
    WAIT_VM(0); BARRIER();
    return;
  }
  {
    const int wm = w >> 1, wn = w & 1;
    const int mrow = 32 * wm + (lane & 31), hh = lane >> 5;
    unsigned xoff[4];
#pragma unroll
    for (int ks = 0; ks < 4; ++ks) xoff[ks] = sbase + mrow * 128 + (((2 * ks + hh) ^ ((mrow >> 1) & 7)) << 4);
    const int g4 = (lane >> 4) & 1, q = (lane & 15) >> 2, p = lane & 3;
    const unsigned woff = sbase + G2_BR + (8 * hh + q) * 128 + (((2 * wn + (p & 1)) ^ (q & 2)) * 32) + 8 * (2 * g4 + (p >> 1));
    f32x16 acc = {};
    struct G2Frags { Frag x[4]; Frag w[4]; } FA, FB;
#define G2_RD(S, sa, sb) do { _Pragma("unroll") for (int ks = 0; ks < 4; ++ks) { \
      DS_B128(S.x[ks].u, xoff[ks] + (sa), 0); DS_TR(S.w[ks].s.a, woff + (sb), ks * 2048); DS_TR(S.w[ks].s.b, woff + (sb), ks * 2048 + 512); } } while (0)
#define G2_MM(S) do { _Pragma("unroll") for (int ks = 0; ks < 4; ++ks) MFMA32_BF16(acc, S.w[ks].u, S.x[ks].u); } while (0)
#define G2_RD1(S, ks, sa, sb) do { DS_B128(S.x[ks].u, xoff[ks] + (sa), 0); DS_TR(S.w[ks].s.a, woff + (sb), ks * 2048); DS_TR(S.w[ks].s.b, woff + (sb), ks * 2048 + 512); } while (0)
#define G2_BAR() do { asm volatile("" ::: "memory"); __builtin_amdgcn_s_barrier(); asm volatile("" ::: "memory"); SCHED(); } while (0)
#define G2_IT(CUR, NXT) do { \
    WAIT_LGKM(9); SCHED(); MFMA32_BF16(acc, CUR.w[0].u, CUR.x[0].u); G2_RD1(NXT, 0, an, bn); SCHED(); \
    WAIT_LGKM(9); SCHED(); MFMA32_BF16(acc, CUR.w[1].u, CUR.x[1].u); G2_RD1(NXT, 1, an, bn); SCHED(); \
    WAIT_LGKM(9); SCHED(); MFMA32_BF16(acc, CUR.w[2].u, CUR.x[2].u); G2_RD1(NXT, 2, an, bn); SCHED(); \
    WAIT_LGKM(9); SCHED(); MFMA32_BF16(acc, CUR.w[3].u, CUR.x[3].u); G2_RD1(NXT, 3, an, bn); SCHED(); \
    an = (an == (G2_NA - 1) * 8192) ? 0u : an + 8192; bn = (bn == (G2_NB - 1) * 8192) ? 0u : bn + 8192; } while (0)
    const size_t go = (size_t)(g2m0 + mrow) * D_ + g2n0 + 32 * wn + 16 * hh;
    u32x4 xa, xb;
    asm volatile("global_load_dwordx4 %0, %2, off\n\tglobal_load_dwordx4 %1, %2, off offset:16" : "=&v"(xa), "=&v"(xb) : "v"(A.x + go) : "memory");
    WAIT_VM(2);
    if (lane == 0) set_flag(sflag, ep);
    BARRIER(); SCHED();

    G2_RD(FA, 0u, 0u);
    SCHED();
    unsigned an = 8192, bn = 8192;
#pragma unroll 1
    for (int i = 0; i < 13; ++i) { G2_BAR(); G2_IT(FA, FB); G2_IT(FB, FA); }
    G2_BAR();
    G2_IT(FA, FB);
    WAIT_LGKM(0); SCHED();
    G2_MM(FB);
    MFMA_DRAIN();
    SCHED();

    asm volatile("s_waitcnt vmcnt(0)" : "+v"(xa), "+v"(xb) :: "memory");
    BARRIER();
    float o[16];
#pragma unroll
    for (int a = 0; a < 4; ++a) {
      const s16x4 sv = trread(smem + ST_OFF + (32 * wn + 16 * hh + 4 * a + q) * 144 + (32 * wm + 16 * g4 + 4 * p) * 2);
#pragma unroll
      for (int i = 0; i < 4; ++i) {
        const int e = 4 * a + i;
        const float ov = bfr(acc[e]);
        const float gv = bfr(ov * __builtin_amdgcn_rcpf(1.0f + __builtin_amdgcn_exp2f(-1.4426950408889634f * ov)));
        const float sf = bf2f((unsigned)(u16)sv[i]);
        const unsigned xw = e < 8 ? xa[e >> 1] : xb[(e - 8) >> 1];
        const float xf = (e & 1) ? bfhi(xw) : bflo(xw);
        o[e] = xf + bfr(sf * gv);
      }
    }
    u32x4 va, vb;
#pragma unroll
    for (int j = 0; j < 4; ++j) { va[j] = pkbf(o[2 * j], o[2 * j + 1]); vb[j] = pkbf(o[8 + 2 * j], o[9 + 2 * j]); }
    *(u32x4*)(A.out + go) = va;
    *(u32x4*)(A.out + go + 8) = vb;

  }
}

#if defined(__HIP_DEVICE_COMPILE__)
#pragma clang attribute pop
#endif

extern "C" void kernel_launch(void* const* d_in, const int* in_sizes, int n_in, void* d_out, int out_size,
                              void* d_ws, size_t ws_size, hipStream_t stream) {
  char* ws = (char*)d_ws;
  KFArgs a;
  a.x = (const u16*)d_in[0]; a.lAr = (const u16*)d_in[1]; a.Aim = (const u16*)d_in[2]; a.Cr = (const u16*)d_in[3];
  a.Ci = (const u16*)d_in[4]; a.ldt = (const u16*)d_in[5]; a.dsk = (const u16*)d_in[6];
  a.wg = (const u16*)d_in[8]; a.wu = (const u16*)d_in[9]; a.wd = (const u16*)d_in[10];
  a.uT = (u16*)(ws + OFF_UT);
  a.h = (u16*)(ws + OFF_H); a.sT = (u16*)(ws + OFF_ST); a.fl = (u64*)(ws + OFF_FL); a.out = (u16*)d_out;
  (void)hipFuncSetAttribute((const void*)kf_main, hipFuncAttributeMaxDynamicSharedMemorySize, KF_LDS);
  kf_main<<<256, 1024, KF_LDS, stream>>>(a);
}
```

```cpp
#include <hip/hip_runtime.h>
#include <hip/hip_bf16.h>
#include <stdint.h>

#if defined(__HIP_DEVICE_COMPILE__)
#pragma clang attribute push(__attribute__((target("no-packed-fp32-ops"))), apply_to = function)
#endif

#define U2F(x) __builtin_bit_cast(float, (unsigned)(x))
#define F2U(x) __builtin_bit_cast(unsigned, (float)(x))
#define TIDX ((int)__builtin_amdgcn_workitem_id_x())
#define BIDX ((int)__builtin_amdgcn_workgroup_id_x())

typedef unsigned short u16;
typedef __attribute__((ext_vector_type(4))) short s16x4;
typedef __attribute__((ext_vector_type(8))) __bf16 bf16x8;
typedef __attribute__((ext_vector_type(8))) _Float16 f16x8;
typedef __attribute__((ext_vector_type(2))) _Float16 f16x2;
typedef __attribute__((ext_vector_type(2))) __fp16 h16x2;
typedef __attribute__((ext_vector_type(4))) float f32x4;
typedef __attribute__((ext_vector_type(2))) float f32x2;
typedef __attribute__((ext_vector_type(16))) float f32x16;
typedef __attribute__((ext_vector_type(4))) unsigned u32x4;
typedef __attribute__((ext_vector_type(2))) unsigned u32x2;
#define LAS __attribute__((address_space(3)))
#define GAS __attribute__((address_space(1)))

constexpr int L_ = 2048, D_ = 512, N_ = 128, F_ = 1792;
constexpr int HP_ = 1856;
constexpr int KZ_ = 2080;

constexpr size_t OFF_FL = 0;
constexpr size_t OFF_UT = 256u << 10;
constexpr size_t OFF_ST = OFF_UT + (2u << 20);
constexpr size_t OFF_H = OFF_ST + (2u << 20);

__device__ __forceinline__ float bf2f(unsigned v) { return U2F(v << 16); }
__device__ __forceinline__ float bflo(unsigned v) { return U2F(v << 16); }
__device__ __forceinline__ float bfhi(unsigned v) { return U2F(v & 0xffff0000u); }
__device__ __forceinline__ float bfr(float f) { return (float)(__bf16)f; }
__device__ __forceinline__ unsigned pkbf(float a, float b) {
  typedef __attribute__((ext_vector_type(2))) __bf16 bf16x2;
  bf16x2 v; v.x = (__bf16)a; v.y = (__bf16)b;
  return __builtin_bit_cast(unsigned, v);
}
__device__ __forceinline__ unsigned pkh(float a, float b) {
  f16x2 v; v.x = (_Float16)a; v.y = (_Float16)b;
  return __builtin_bit_cast(unsigned, v);
}
__device__ __forceinline__ void glds16(const void* g, LAS void* l) {
  __builtin_amdgcn_global_load_lds((const GAS void*)g, l, 16, 0, 0);
}
__device__ __forceinline__ void glds16_sc1(const void* g, LAS void* l) {
  __builtin_amdgcn_global_load_lds((const GAS void*)g, l, 16, 0, 16);
}
__device__ __forceinline__ s16x4 trread(LAS char* p) {
  return __builtin_amdgcn_ds_read_tr16_b64_v4i16((LAS s16x4*)p);
}
union Frag { struct { s16x4 a, b; } s; bf16x8 bf; f16x8 h; u32x4 u; };

#define WAIT_VM(n) asm volatile("s_waitcnt vmcnt(" #n ")" ::: "memory")
#define WAIT_VMN(n) asm volatile("s_waitcnt vmcnt(%0)" :: "i"(n) : "memory")
#define BARRIER() do { asm volatile("s_waitcnt lgkmcnt(0)" ::: "memory"); __builtin_amdgcn_s_barrier(); asm volatile("" ::: "memory"); } while (0)

#define DS_TR(dst, addr, off) asm volatile("ds_read_b64_tr_b16 %0, %1 offset:%2" : "=v"(dst) : "v"(addr), "i"(off))
#define DS_B128(dst, addr, off) asm volatile("ds_read_b128 %0, %1 offset:%2" : "=v"(dst) : "v"(addr), "i"(off))
#define WAIT_LGKM(n) asm volatile("s_waitcnt lgkmcnt(" #n ")" ::: "memory")
#define SCHED() __builtin_amdgcn_sched_barrier(0)
#define MFMA32_BF16(acc, a, b) asm volatile("v_mfma_f32_32x32x16_bf16 %0, %1, %2, %0" : "+v"(acc) : "v"(a), "v"(b))
#define MFMA_DRAIN() asm volatile("s_nop 15\n\ts_nop 7" ::: "memory")

__device__ __forceinline__ float wave_sum(float v) {
  v += __builtin_bit_cast(float, __builtin_amdgcn_update_dpp(0, __builtin_bit_cast(int, v), 0xB1, 0xf, 0xf, true));
  v += __builtin_bit_cast(float, __builtin_amdgcn_update_dpp(0, __builtin_bit_cast(int, v), 0x4E, 0xf, 0xf, true));
  v += __builtin_bit_cast(float, __builtin_amdgcn_update_dpp(0, __builtin_bit_cast(int, v), 0x141, 0xf, 0xf, true));
  v += __builtin_bit_cast(float, __builtin_amdgcn_update_dpp(0, __builtin_bit_cast(int, v), 0x140, 0xf, 0xf, true));
  const float r0 = __builtin_bit_cast(float, __builtin_amdgcn_readlane(__builtin_bit_cast(int, v), 0));
  const float r1 = __builtin_bit_cast(float, __builtin_amdgcn_readlane(__builtin_bit_cast(int, v), 16));
  const float r2 = __builtin_bit_cast(float, __builtin_amdgcn_readlane(__builtin_bit_cast(int, v), 32));
  const float r3 = __builtin_bit_cast(float, __builtin_amdgcn_readlane(__builtin_bit_cast(int, v), 48));
  return (r0 + r1) + (r2 + r3);
}
__device__ __forceinline__ void cexp_p(float arl2, float wh, float wl, float p, float& re, float& im) {
  const float mag = __builtin_amdgcn_exp2f(arl2 * p);
  const float fr = __builtin_fmaf(wl, p, __builtin_amdgcn_fractf(wh * p));
  re = mag * __builtin_amdgcn_cosf(fr);
  im = mag * __builtin_amdgcn_sinf(fr);
}
__device__ __forceinline__ void split_h(float a, float b, unsigned& hi, unsigned& lo) {
  h16x2 h = __builtin_amdgcn_cvt_pkrtz(a, b);
  float ra = a - (float)h.x, rb = b - (float)h.y;
  h16x2 l = __builtin_amdgcn_cvt_pkrtz(ra, rb);
  hi = __builtin_bit_cast(unsigned, h);
  lo = __builtin_bit_cast(unsigned, l);
}

constexpr int G1_A = 16384, G1_W = 32768, G1_STAGE = G1_A + G1_W;
constexpr int KF_LDS = 163840;
constexpr int HS_PART = 98304;
constexpr int HS_CS = 147456;
constexpr int HS_ST = HS_CS + 1024;
constexpr int HS_PRM = 158720;
constexpr int CV_KS = 4672, CV_US = 4736, CV_CH = 4 * CV_KS + 2 * CV_US;
constexpr int KB2 = 64, KB4 = 64 + CV_KS, KB6 = 64 + 2 * CV_KS, KB0 = 64 + 3 * CV_KS - 16;
constexpr int UB0 = 4 * CV_KS + 64, UB1 = UB0 + CV_US;
constexpr int CV_XCH = 57344;
constexpr int G2_BR = 81920, G2_NB = 10, G2_NA = 10;
constexpr int ST_OFF = 2 * 8192;
constexpr int FL_LN = 0, FL_H = 2048, FL_S = 4096, FL_U = 6144, FL_CS = 8192;

__device__ __forceinline__ float gelu_t(float x) {
  const float c1 = -2.0f * 0.7978845608028654f * 1.4426950408889634f;
  const float c2 = c1 * 0.044715f;
  float a = x * __builtin_fmaf(x * x, c2, c1);
  float e = __builtin_amdgcn_exp2f(a);
  return x * __builtin_amdgcn_rcpf(1.0f + e);
}
typedef unsigned long long u64;
extern "C" __device__ u64 kf_dispatch_id() __asm("llvm.amdgcn.dispatch.id");
__device__ __forceinline__ u64 launch_epoch() {
  const u64 x = kf_dispatch_id() * 0x9E3779B97F4A7C15ull + (u64)(size_t)__builtin_amdgcn_dispatch_ptr() * 0xC2B2AE3D27D4EB4Full;
  return (x ^ (x >> 29)) | 0x8000000000000001ull;
}
__device__ __forceinline__ void set_flag(u64* p, u64 e) { __hip_atomic_store((GAS u64*)p, e, __ATOMIC_RELAXED, __HIP_MEMORY_SCOPE_AGENT); }
__device__ __forceinline__ void put_gran(u64* p, unsigned v, u64 e) { set_flag(p, (e & 0xffffffff00000000ull) | v); }
__device__ __forceinline__ unsigned get_gran(const u64* p, u64 g, u64 e) {
  for (unsigned spins = 0; spins < (1u << 20) && __builtin_amdgcn_ballot_w64((g >> 32) != (e >> 32)) != 0ull; ++spins) {
    __builtin_amdgcn_s_sleep(1);
    g = __hip_atomic_load((GAS u64*)p, __ATOMIC_RELAXED, __HIP_MEMORY_SCOPE_AGENT);
  }
  return (unsigned)g;
}
__device__ __forceinline__ void wait_flags2(const u64* p0, const u64* p1, u64 e) {
  for (unsigned spins = 0; spins < (1u << 20); ++spins) {
    const u64 a = __hip_atomic_load((GAS u64*)p0, __ATOMIC_RELAXED, __HIP_MEMORY_SCOPE_AGENT);
    const u64 b = __hip_atomic_load((GAS u64*)p1, __ATOMIC_RELAXED, __HIP_MEMORY_SCOPE_AGENT);
    if (__builtin_amdgcn_ballot_w64(a != e || b != e) == 0ull) break;
    __builtin_amdgcn_s_sleep(1);
  }
}

struct KIdx { u16 v[2304]; };
constexpr KIdx make_kidx() {
  KIdx t{};
  for (int x = 0; x < 2304; ++x) {
    const int idx = KZ_ - x;
    int o = 21 * 32 + 1;
    if (idx >= 0 && idx < L_) {
      int p = idx;
      int sh = idx >= 1024 ? 3 : (idx >= 512 ? 2 : (idx >= 256 ? 1 : 0));
      if (sh) {
        const int q = idx >> sh, rem = idx & ((1 << sh) - 1), half = 1 << (sh - 1);
        p = (q + ((rem > half || (rem == half && (q & 1))) ? 1 : 0)) << sh;
      }
      const int r = p & 7, cc = p >> 3, c1 = cc >> 5, c0 = cc & 31;
      const int rw = c1 == 0 ? r : (c1 == 1 ? 8 + (r >> 1) : (c1 < 4 ? 8 + 2 * c1 + (r >> 2) : 12 + c1));
      o = rw * 32 + c0;
    }
    t.v[x] = (u16)(o * 4);
  }
  return t;
}
__device__ const KIdx g_kidx = make_kidx();

struct G1Frags { Frag x[2]; Frag w[2]; };
struct KFArgs {
  const u16 *x, *lAr, *Aim, *Cr, *Ci, *ldt, *dsk, *wg, *wu, *wd;
  u16 *uT, *h, *sT; u64* fl; u16* out;
};

__device__ __forceinline__ unsigned mov32(unsigned x) { unsigned r; asm("v_mov_b32 %0, %1" : "=v"(r) : "v"(x)); return r; }
__device__ __forceinline__ void split_h2(float a, float b, unsigned& hi, unsigned& lo) {
  const h16x2 h = __builtin_amdgcn_cvt_pkrtz(a, b);
  f16x2 l;
  l.x = (_Float16)__builtin_fmaf((float)h.x, -1.0f, a);
  l.y = (_Float16)__builtin_fmaf((float)h.y, -1.0f, b);
  hi = __builtin_bit_cast(unsigned, h);
  lo = __builtin_bit_cast(unsigned, l);
}
#define HBAR() do { SCHED(); asm volatile("" ::: "memory"); __builtin_amdgcn_s_barrier(); asm volatile("" ::: "memory"); SCHED(); } while (0)
__device__ __forceinline__ void kf_helper(LAS char* smem, const KFArgs& A, int j, int lane, int g2m0, int g2n0, u64 ep, bool has_tile, int tmt, int tn0, int tkb) {
  const int b = BIDX, ch = j >> 2, nq = j & 3, d = 2 * b + ch;
  LAS float* prm = (LAS float*)(smem + HS_PRM) + j * 160;
  LAS float* part = (LAS float*)(smem + HS_PART) + (j >> 1) * 704;
  const int uc = 64 * nq + lane;
  unsigned warm = 0, warm2 = 0;
  const int ht = j * 64 + lane, bi = b >> 3;
#define TOUCH(reg, ptr) asm volatile("global_load_dword %0, %1, off" : "+v"(reg) : "v"(ptr) : "memory")
  if (j == 7) {
    const char* tp;
    if (lane < 16) tp = (const char*)(A.h + (size_t)(tmt * 128 + 8 * lane) * HP_ + F_);
    else if (lane < 24) tp = (const char*)(A.h + (size_t)(g2m0 + 8 * (lane - 16)) * HP_ + F_);
    else if (lane < 52) tp = (const char*)(A.wd + (size_t)(64 * (lane - 24)) * D_ + g2n0);
    else if (lane < 56) tp = (const char*)(A.out + (size_t)(g2m0 + 16 * (lane - 52)) * D_ + g2n0);
    else tp = (const char*)(A.dsk + d);
    TOUCH(warm2, tp);
  }
  const u32x4 kx0 = *(const u32x4*)(g_kidx.v + 8 * (4 + uc));
  const int c2 = lane < 4 ? lane : 256 + (lane < 21 ? lane : 20);
  const u32x4 kx1 = *(const u32x4*)(g_kidx.v + 8 * c2);
  float kinv;
  u32x2 xt[4] = {};
  {
    const int n = d * N_ + nq * 32 + (lane & 31);
    const u32x4 xv = *(const u32x4*)(A.x + (size_t)(b * 8 + j) * D_ + lane * 8);
    const unsigned vdt = A.ldt[d];
    const unsigned a = *(const unsigned*)(A.Cr + d * N_ + 2 * lane), bb = *(const unsigned*)(A.Ci + d * N_ + 2 * lane);
    const unsigned v0 = A.lAr[n], v1 = A.Aim[n], v2 = A.Cr[n], v3 = A.Ci[n];
    HBAR();
    if (tkb >= 0) {
      const int q = (lane & 15) >> 2, p = lane & 3, chk = 2 * (j & 3) + (p >> 1);
#pragma unroll
      for (int t = 0; t < 4; ++t) {
        const int ra = 64 * (j >> 2) + 32 * (t >> 1) + 8 * (lane >> 4) + 4 * (t & 1) + q;
        xt[t] = __builtin_bit_cast(u32x2, trread(smem + ra * 128 + ((chk ^ ((ra >> 1) & 7)) << 4) + 8 * (p & 1)));
      }
    }
    {
      float f[8];
#pragma unroll
      for (int e = 0; e < 4; ++e) { f[2 * e] = bflo(xv[e]); f[2 * e + 1] = bfhi(xv[e]); }
      const float mean = wave_sum(((f[0] + f[1]) + (f[2] + f[3])) + ((f[4] + f[5]) + (f[6] + f[7]))) * (1.0f / D_);
      float q2 = 0.f;
#pragma unroll
      for (int e = 0; e < 8; ++e) { const float dd = f[e] - mean; q2 = __builtin_fmaf(dd, dd, q2); }
      const float var = wave_sum(q2) * (1.0f / D_);
      const float sd = __builtin_sqrtf(bfr(bfr(var) + 1e-5f));
      if (lane == 0) put_gran(A.fl + FL_LN + b * 8 + j, pkbf(mean, sd), ep);
    }
    const float dt = bfr(__builtin_expf(bf2f(vdt)));
    const float sb = wave_sum((__builtin_fabsf(bflo(a)) + __builtin_fabsf(bfhi(a))) + (__builtin_fabsf(bflo(bb)) + __builtin_fabsf(bfhi(bb)))) * dt * 2.0f;
    const int ex = (int)((F2U(sb) >> 23) & 0xff) - 127;
    int se = 14 - ex;
    se = se > 100 ? 100 : (se < -100 ? -100 : se);
    const float cs = dt * 2.0f * U2F((unsigned)(127 + se) << 23);
    kinv = U2F((unsigned)(127 - se) << 23);
    const float ar = -bfr(__builtin_expf(bf2f(v0))) * dt;
    const float ai = bf2f(v1) * dt;
    const float w0 = ai * 0.15915494309189535f;
    const float wh = U2F(F2U(w0) & 0xfffff000u);
    const float wl = __builtin_fmaf(ai, 6.4206383e-09f, __builtin_fmaf(ai, 0.15915494309189535f, -wh));
    if (lane < 32) {
      prm[lane] = ar * 1.4426950408889634f; prm[32 + lane] = wh; prm[64 + lane] = wl;
      prm[96 + lane] = bf2f(v2) * cs; prm[128 + lane] = bf2f(v3) * cs;
    }
  }
  const int row = lane & 31, hh = lane >> 5;
  int prow;
  if (row < 8) prow = row;
  else if (row < 12) prow = 256 + 2 * (row - 8);
  else if (row < 16) prow = 512 + 256 * ((row - 12) >> 1) + 4 * ((row - 12) & 1);
  else prow = 1024 + 256 * (row - 16);
  prow = row < 21 ? prow : 0;
  const float pA = (float)prow, pB = (float)(8 * row);
  f32x16 acc = {};
  const int pnl = 4 * nq + ((lane & 7) < 4 ? (lane & 7) : 4);
  const u64* f0 = A.fl + FL_U + (pnl < 16 ? pnl : 15) * 64 + (d >> 6) * 8 + ((d >> 4) & 3) + 4 * ((lane >> 3) & 1);
  const u64* gp = j < 2 ? A.fl + FL_LN + tmt * 128 + ht : A.fl + FL_CS + ((j >> 1) & 1) * F_ + tn0 + (j & 1) * 64 + lane;
  u64 fa = 0, gr = 0;
  typedef __attribute__((ext_vector_type(4))) _Float16 f16x4;
#pragma unroll 1
  for (int hs = 0; hs < 8; ++hs) {
    if (hs == 3 || hs == 5 || hs == 7) {
      asm volatile("s_waitcnt vmcnt(0)" : "+v"(gr) :: "memory");
      if (hs == 3 && has_tile && j < 2) {
        const unsigned g = get_gran(gp, gr, ep);
        *(LAS f32x2*)(smem + HS_ST + ht * 8) = (f32x2){bflo(g), __builtin_amdgcn_rcpf(bfhi(g))};
      }
      if (hs == 5 && tkb >= 0 && lane == 0) set_flag(A.fl + FL_U + (tmt * 8 + tkb) * 8 + j, ep);
      if (hs == 7) {
        if (has_tile && j >= 4) *(LAS float*)(smem + HS_CS + (ht - 256) * 4) = U2F(get_gran(gp, gr, ep));
        asm volatile("global_load_dwordx2 %0, %1, off sc1" : "+v"(fa) : "v"(f0) : "memory");
      }
    }

    BARRIER();
    if (has_tile && ((hs == 1 && j < 2) || (hs == 5 && j >= 4)))
      asm volatile("global_load_dwordx2 %0, %1, off sc1" : "+v"(gr) : "v"(gp) : "memory");
    if (hs == 3 && tkb >= 0) {
      const __amdgpu_buffer_rsrc_t ru = __builtin_amdgcn_make_buffer_rsrc(A.uT, 0, L_ * D_ * 2, 0x00020000);
#pragma unroll
      for (int t = 0; t < 2; ++t) {
        const int r0 = 64 * (j >> 2) + 32 * t + 8 * (lane >> 4);
        u32x4 o;
#pragma unroll
        for (int e = 0; e < 4; ++e) {
          const f32x4 st = *(const LAS f32x4*)(smem + HS_ST + (r0 + 2 * e) * 8);
          const unsigned xw = xt[2 * t + (e >> 1)][e & 1];
          const unsigned tt = pkbf(bflo(xw) - st[0], bfhi(xw) - st[2]);
          const unsigned oo = pkbf(bflo(tt) * st[1], bfhi(tt) * st[3]);
          o[e] = __builtin_bit_cast(unsigned, __builtin_amdgcn_cvt_pkrtz(bflo(oo), bfhi(oo)));
        }
        __builtin_amdgcn_raw_buffer_store_b128(o, ru, (unsigned)((64 * tkb + 16 * (j & 3) + (lane & 15)) * L_ + 128 * tmt + r0) * 2u, 0, 16);
      }
    }
    const int n0 = hs * 4 + hh * 2;
    const f32x2 va = *(LAS f32x2*)(prm + n0), vh = *(LAS f32x2*)(prm + 32 + n0), vl = *(LAS f32x2*)(prm + 64 + n0);
    const f32x2 vr = *(LAS f32x2*)(prm + 96 + n0), vi = *(LAS f32x2*)(prm + 128 + n0);
    u32x2 Ah, Al, Bh, Bl;
#pragma unroll
    for (int q = 0; q < 2; ++q) {
      float er, ei, yr, yi;
      cexp_p(va[q], vh[q], vl[q], pA, er, ei);
      cexp_p(va[q], vh[q], vl[q], pB, yr, yi);
      const float xr = vr[q] * er - vi[q] * ei, xi = vr[q] * ei + vi[q] * er;
      unsigned h0, l0, h1, l1;
      split_h(xr, xi, h0, l0);
      split_h(yr, -yi, h1, l1);
      Ah[q] = h0; Al[q] = l0; Bh[q] = h1; Bl[q] = l1;
    }
    acc = __builtin_amdgcn_mfma_f32_32x32x8f16(__builtin_bit_cast(f16x4, Al), __builtin_bit_cast(f16x4, Bh), acc, 0, 0, 0);
    acc = __builtin_amdgcn_mfma_f32_32x32x8f16(__builtin_bit_cast(f16x4, Ah), __builtin_bit_cast(f16x4, Bl), acc, 0, 0, 0);
    acc = __builtin_amdgcn_mfma_f32_32x32x8f16(__builtin_bit_cast(f16x4, Ah), __builtin_bit_cast(f16x4, Bh), acc, 0, 0, 0);
  }
  if ((j & 1) == 0) {
#pragma unroll
    for (int i = 0; i < 12; ++i) {
      const int rr = (i & 3) + 8 * (i >> 2) + 4 * hh;
      if (rr < 21) part[rr * 32 + row] = acc[i];
    }
    if (lane == 0) part[21 * 32 + 1] = 0.f;
  }
  BARRIER();
  asm volatile("s_waitcnt vmcnt(0)" : "+v"(fa) :: "memory");
  if (__builtin_amdgcn_ballot_w64(fa != ep) != 0ull) wait_flags2(f0, f0, ep);
  u32x4 ua = {0u, 0u, 0u, 0u}; unsigned unx = 0;
  {
    const u16* ur = A.uT + (size_t)d * L_ + 8 * uc;
    asm volatile("global_load_dwordx4 %0, %2, off sc1\n\tglobal_load_ushort %1, %3, off sc1" : "+v"(ua), "+v"(unx) : "v"(ur), "v"(ur + (uc < 255 ? 8 : 0)) : "memory");
  }
  if (j & 1) {
#pragma unroll
    for (int i = 0; i < 12; ++i) {
      const int rr = (i & 3) + 8 * (i >> 2) + 4 * hh;
      if (rr < 21) part[rr * 32 + row] += acc[i];
    }
  }
  BARRIER();
  {
    const LAS char* t0 = smem + HS_PART + (2 * ch) * 2816;
    LAS char* cbase = smem + ch * CV_CH;
    auto expand = [&](const u32x4& kx, int c) {
      float v[8];
#pragma unroll
      for (int e = 0; e < 8; ++e) {
        const unsigned o = (e & 1) ? (kx[e >> 1] >> 16) : (kx[e >> 1] & 0xffffu);
        v[e] = *(const LAS float*)(t0 + o) + *(const LAS float*)(t0 + o + 2816);
      }
      u32x4 o;
#pragma unroll
      for (int e = 0; e < 4; ++e) o[e] = pkh(v[2 * e], v[2 * e + 1]);
      *(LAS u32x4*)(cbase + KB0 + 16 * c) = o;
      LAS unsigned* q2 = (LAS unsigned*)(cbase + KB2 + 16 * c - 4);
      LAS u32x2* q4 = (LAS u32x2*)(cbase + KB4 + 16 * c - 8);
      LAS unsigned* q6 = (LAS unsigned*)(cbase + KB6 + 16 * c - 12);
#pragma unroll
      for (int e = 0; e < 4; ++e) { q2[e] = o[e]; q6[e] = o[e]; }
      q4[0] = (u32x2){o[0], o[1]}; q4[1] = (u32x2){o[2], o[3]};
    };
    expand(kx0, 4 + uc);
    if (nq == 0) {
      if (lane < 21) expand(kx1, c2);
      if (lane == 0) *(LAS float*)(cbase) = kinv;
    }
    asm volatile("s_waitcnt vmcnt(0)" : "+v"(ua), "+v"(unx) :: "memory");
    const unsigned nx = (uc < 255) ? unx : 0u;
    const int P = uc + 2 * (uc >> 4) + 4;
    *(LAS u32x4*)(cbase + UB0 + 16 * P) = ua;
    u32x4 s1;
    s1[0] = (ua[0] >> 16) | (ua[1] << 16); s1[1] = (ua[1] >> 16) | (ua[2] << 16);
    s1[2] = (ua[2] >> 16) | (ua[3] << 16); s1[3] = (ua[3] >> 16) | (nx << 16);
    *(LAS u32x4*)(cbase + UB1 + 16 * P) = s1;
    if (uc == 0) {
      const u32x4 z = {0u, 0u, 0u, 0u};
      *(LAS u32x4*)(cbase + UB0) = z; *(LAS u32x4*)(cbase + UB0 + 16) = z;
      *(LAS u32x4*)(cbase + UB1) = z; *(LAS u32x4*)(cbase + UB1 + 16) = (u32x4){0u, 0u, 0u, ua[0] << 16};
    }
  }

  BARRIER();

  {
    constexpr int NST = F_ / 64;
    const char* bbase = (const char*)(A.wd + g2n0);
    const int r = 8 * j + (lane >> 3);
    const unsigned boff = (unsigned)(r * D_) * 2u + (unsigned)((((lane & 7) >> 1) ^ (r & 2)) * 32 + (lane & 1) * 16);
    const char* bp = bbase + boff;
    unsigned slot = 0;
    TOUCH(warm, (const char*)A.wd + (size_t)(bi * 448 + (ht < 448 ? ht : 447)) * 128);
#pragma unroll 1
    for (int s = 0; s < 8; ++s) { glds16(bp, smem + G2_BR + slot + j * 1024); bp += 128 * D_; slot += 8192; }
    BARRIER();
    if (j == 0) { const u64* hf = A.fl + FL_H + (g2m0 >> 7) * 128; wait_flags2(hf + lane, hf + (lane < 48 ? 64 + lane : lane), ep); }
    BARRIER();
    {
      const int t = (g2n0 >> 6) * 224 + (ht < 224 ? ht : 223);
      const int ln = t >> 6, r = t & 63;
      TOUCH(warm2, (const char*)A.h + (size_t)(g2m0 + r) * (HP_ * 2) + ln * 128);
    }
    asm volatile("s_waitcnt vmcnt(8)" : "+v"(warm) :: "memory");
    BARRIER();
    const u64* sf = A.fl + FL_S + (g2n0 >> 6) * 256 + 32 * j + (lane & 31);
    u64 sflag = 0;
    u32x4 sv = {0u, 0u, 0u, 0u};
    const int sc = j * 64 + lane;
#pragma unroll 1
    for (int i = 0; i < 10; ++i) {
      WAIT_VM(5);
      BARRIER();
      if (i == 6) asm volatile("global_load_dwordx2 %0, %1, off sc1" : "+v"(sflag) : "v"(sf) : "memory");
      glds16(bp, smem + G2_BR + slot + j * 1024);
      glds16(bp + 128 * D_, smem + G2_BR + slot + 8192 + j * 1024);
      bp += 256 * D_;
      slot = (slot == 8 * 8192) ? 0u : slot + 16384;
    }
    asm volatile("s_waitcnt vmcnt(5)" : "+v"(sflag) :: "memory");
    BARRIER();
    {
      if (__builtin_amdgcn_ballot_w64(sflag != ep) != 0ull) wait_flags2(sf, sf, ep);
      const __amdgpu_buffer_rsrc_t rs = __builtin_amdgcn_make_buffer_rsrc(A.sT, 0, D_ * L_ * 2, 0x00020000);
      const unsigned so = (unsigned)(((g2n0 + (sc >> 3)) * L_ + g2m0 + 8 * (sc & 7)) * 2);
      asm volatile("buffer_load_dwordx4 %0, %1, %2, 0 offen sc1" : "+v"(sv) : "v"(so), "s"(rs) : "memory");
    }
    WAIT_VM(4); BARRIER();
    WAIT_VM(2); BARRIER();
    WAIT_VM(1); BARRIER();
    asm volatile("s_waitcnt vmcnt(0)" : "+v"(sv), "+v"(warm2) :: "memory");
    *(LAS u32x4*)(smem + ST_OFF + (sc >> 3) * 144 + (sc & 7) * 16) = sv;
    BARRIER();
  }
}

__global__ __launch_bounds__(1024, 4) void kf_main(KFArgs A) {
  extern __shared__ __attribute__((aligned(16))) char smem_[];
  LAS char* smem = (LAS char*)smem_;
  const unsigned sbase = (unsigned)(size_t)smem;
  const int tid = TIDX, lane = tid & 63;
  const int w = __builtin_amdgcn_readfirstlane(tid >> 6);
  const int b = BIDX, xcd = b & 7, idx = b >> 3;
  const bool has_tile = idx < 28;
  const int g2mt = xcd * 4 + (idx & 3), g2nt = idx >> 2;
  const int g2m0 = g2mt * 64, g2n0 = g2nt * 64;

  const u64 ep = launch_epoch();
  const int mt = (xcd & 3) * 4 + (idx & 3), nt = (xcd >> 2) * 7 + (has_tile ? idx >> 2 : 6);
  const int rot = nt & 7;
  if (w >= 8) { kf_helper(smem, A, w - 8, lane, g2m0, g2n0, ep, has_tile, mt, nt * 128, (has_tile && nt < 8) ? (nt & 7) : -1); return; }

  const int m0 = mt * 128, n0 = nt * 128;
  u64* hflag = A.fl + FL_H + mt * 128 + nt * 8 + w;
  const char* abase = (const char*)(A.x + (size_t)m0 * D_);
  const char* gbase = (const char*)(A.wg + n0);
  const char* ubase = (const char*)(A.wu + n0);
  const int arow = 16 * w + (lane >> 3);
  const unsigned aoff0 = (unsigned)(arow * D_ + 8 * ((lane & 7) ^ ((arow >> 1) & 7))) * 2u;
  const unsigned aoff1 = (unsigned)((arow + 8) * D_ + 8 * ((lane & 7) ^ (((arow + 8) >> 1) & 7))) * 2u;
  const int wk = 8 * w + (lane >> 3);
  const int fk = (wk >> 1) & 1;
  const unsigned woffg = (unsigned)(wk * F_) * 2u + (unsigned)((((lane & 7) >> 1) ^ fk) * 32 + (lane & 1) * 16);
  const unsigned woffu = (unsigned)(wk * F_) * 2u + (unsigned)((((lane & 7) >> 1) ^ (fk | 2)) * 32 + (lane & 1) * 16);
#define G1_LD(i, s_, sb) do { const int s = ((s_) + rot) & 7; \
    if ((i) == 0) glds16(abase + (s) * 128 + aoff0, smem + (sb) + (2 * w) * 1024); \
    if ((i) == 1) glds16(gbase + (long)(s) * (128 * F_) + woffg, smem + (sb) + G1_A + w * 1024); \
    if ((i) == 2) glds16(gbase + (long)(s) * (128 * F_) + 128 + woffg, smem + (sb) + G1_A + 8192 + w * 1024); \
    if ((i) == 3) glds16(abase + (s) * 128 + aoff1, smem + (sb) + (2 * w + 1) * 1024); \
    if ((i) == 4) glds16(ubase + (long)(s) * (128 * F_) + woffu, smem + (sb) + G1_A + 16384 + w * 1024); \
    if ((i) == 5) glds16(ubase + (long)(s) * (128 * F_) + 128 + woffu, smem + (sb) + G1_A + 24576 + w * 1024); } while (0)
  if (has_tile) {
#pragma unroll
    for (int i = 0; i < 6; ++i) G1_LD(i, 0, 0);
#pragma unroll
    for (int i = 0; i < 6; ++i) G1_LD(i, 1, G1_STAGE);
    const int wm = w & 1, wn = w >> 1;
    const int mrow = 64 * wm + (lane & 31), hh = lane >> 5;
    unsigned xoff[4];
#pragma unroll
    for (int ks = 0; ks < 4; ++ks) xoff[ks] = sbase + mrow * 128 + (((2 * ks + hh) ^ ((mrow >> 1) & 7)) << 4);
    const int mat = (lane >> 4) & 1, q = (lane & 15) >> 2, p = lane & 3;
    const int pp = ((p & 1) << 1) | (p >> 1);
    const int fq = ((q >> 1) & 1) | (mat << 1);
    const unsigned wrow = sbase + G1_A + (mat * 2 + (wn >> 1)) * 8192 + (8 * hh + q) * 128 + pp * 8;
    const unsigned woff0 = wrow + (((2 * (wn & 1)) ^ fq) * 32);
    const unsigned woff1 = wrow + (((2 * (wn & 1) + 1) ^ fq) * 32);
    f32x16 acc[2][2];
#pragma unroll
    for (int t = 0; t < 2; ++t) { acc[t][0] = (f32x16){}; acc[t][1] = (f32x16){}; }
    G1Frags FA, FB;
#define RD_W(S, t, b0, b1, ks) do { DS_TR(S.w[t].s.a, (t) ? b1 : b0, (ks) * 2048); DS_TR(S.w[t].s.b, (t) ? b1 : b0, (ks) * 2048 + 512); } while (0)
#define RD_X(S, XA) do { DS_B128(S.x[0].u, XA, 0); DS_B128(S.x[1].u, XA, 4096); } while (0)
#define MM1(S, t, m) MFMA32_BF16(acc[t][m], S.w[t].u, S.x[m].u)
    WAIT_VM(6);
    BARRIER();

    RD_X(FA, xoff[0]); RD_W(FA, 0, woff0, woff1, 0); RD_W(FA, 1, woff0, woff1, 0);
    SCHED();
    unsigned so = 0, sn = G1_STAGE, sp = 2 * G1_STAGE;
#define KSTEP(CUR, NXT, XA, KSN, L0, MORE) \
    WAIT_LGKM(2); SCHED(); MM1(CUR, 0, 0); RD_X(NXT, XA); SCHED(); \
    MM1(CUR, 0, 1); RD_W(NXT, 0, w0, w1, KSN); if (MORE) G1_LD(L0, st + 2, sp); SCHED(); \
    WAIT_LGKM(4); SCHED(); MM1(CUR, 1, 0); RD_W(NXT, 1, w0, w1, KSN); SCHED(); \
    MM1(CUR, 1, 1); if (MORE) G1_LD(L0 + 1, st + 2, sp); SCHED();
#define G1_BODY(MORE) { \
    const unsigned w0 = woff0 + so, w1 = woff1 + so; \
    const unsigned x1 = xoff[1] + so, x2 = xoff[2] + so, x3 = xoff[3] + so; \
    const unsigned xn0 = xoff[0] + sn, w0n = woff0 + sn, w1n = woff1 + sn; \
    KSTEP(FA, FB, x1, 1, 0, MORE) \
    KSTEP(FB, FA, x2, 2, 2, MORE) \
    KSTEP(FA, FB, x3, 3, 4, MORE) \
    WAIT_LGKM(2); SCHED(); MM1(FB, 0, 0); SCHED(); \
    if (MORE) WAIT_VM(6); else WAIT_VM(0); \
    BARRIER(); \
    SCHED(); \
    RD_X(FA, xn0); \
    MM1(FB, 0, 1); RD_W(FA, 0, w0n, w1n, 0); SCHED(); \
    MM1(FB, 1, 0); RD_W(FA, 1, w0n, w1n, 0); SCHED(); \
    MM1(FB, 1, 1); SCHED(); \
    sp = so; so = sn; sn = (sn == 2 * G1_STAGE) ? 0u : sn + G1_STAGE; }
#pragma unroll 1
    for (int st = 0; st < 6; ++st) G1_BODY(true)
#pragma unroll 1
    for (int st = 6; st < 8; ++st) G1_BODY(false)
    WAIT_LGKM(0);
    MFMA_DRAIN();
    SCHED();

    BARRIER();
    __builtin_amdgcn_s_setprio(2);
    const __amdgpu_buffer_rsrc_t rh = __builtin_amdgcn_make_buffer_rsrc(A.h, 0, L_ * HP_ * 2, 0x00020000);
    const unsigned ho = (unsigned)((m0 + mrow) * HP_ + n0 + 32 * wn + 8 * hh) * 2u;
#pragma unroll
    for (int t = 0; t < 2; ++t) {
      const LAS f32x4* cp = (const LAS f32x4*)(smem + HS_CS + (32 * wn + 16 * t + 8 * hh) * 4);
      const f32x4 cg0 = cp[0], cg1 = cp[1], cu0 = cp[32], cu1 = cp[33];
#pragma unroll
      for (int m = 0; m < 2; ++m) {
        const f32x2 st = *(const LAS f32x2*)(smem + HS_ST + (mrow + 32 * m) * 8);
        const float nm = -st[0] * st[1];
        u32x4 v;
#pragma unroll
        for (int j = 0; j < 4; ++j) {
          float o[2];
#pragma unroll
          for (int e = 0; e < 2; ++e) {
            const int i = 2 * j + e;
            const float g = __builtin_fmaf(acc[t][m][i], st[1], nm * (i < 4 ? cg0[i & 3] : cg1[i & 3]));
            const float u = __builtin_fmaf(acc[t][m][8 + i], st[1], nm * (i < 4 ? cu0[i & 3] : cu1[i & 3]));
            o[e] = gelu_t(g) * u;
          }
          v[j] = pkbf(o[0], o[1]);
        }
        __builtin_amdgcn_raw_buffer_store_b128(v, rh, ho + (unsigned)(m * 32 * HP_ + 16 * t) * 2u, 0, 16);
        if (t == 0 && m == 0) { SCHED(); BARRIER(); SCHED(); }
      }
    }
    __builtin_amdgcn_s_setprio(0);

    BARRIER();
  } else {
    const int q = b - 224;
    const u16* wsrc = (q < 16 ? A.wg : A.wu) + (q & 15) * 112;
    const int c = lane & 15, rs4 = lane >> 4;
    float sum[8] = {};
#pragma unroll
    for (int i = 0; i < 16; ++i) {
      const u32x4 v = *(const u32x4*)(wsrc + (size_t)(64 * w + 4 * i + rs4) * F_ + 8 * (c < 14 ? c : 13));
#pragma unroll
      for (int e = 0; e < 4; ++e) { sum[2 * e] += bflo(v[e]); sum[2 * e + 1] += bfhi(v[e]); }
    }
    LAS float* red = (LAS float*)smem;
#pragma unroll
    for (int e = 0; e < 8; ++e) red[(w * 4 + rs4) * 128 + c * 8 + e] = sum[e];
    BARRIER();
    if (tid < 112) {
      float t = 0.f;
#pragma unroll 8
      for (int i = 0; i < 32; ++i) t += red[i * 128 + tid];
      put_gran(A.fl + FL_CS + (q >> 4) * F_ + (q & 15) * 112 + tid, F2U(t), ep);
    }
#pragma unroll 1
    for (int i = 0; i < 11; ++i) BARRIER();
  }

  {
    const int ch = w >> 2, v4 = (w + 2 * ch) & 3, d = 2 * b + ch;
    LAS char* cbase = smem + ch * CV_CH;
    const unsigned cb = sbase + ch * CV_CH;
    const int r = lane & 15, g = lane >> 4;
    const int e = (-2 * r) & 7;
    const int kb = (e == 0) ? KB0 : (e == 2 ? KB2 : (e == 4 ? KB4 : KB6));
    const int ub = (r & 1) ? UB1 : UB0;
    const int ubv = (int)cb + ub;
    const int ulv = ubv + 16 * (18 * (r >> 1) + g);
    int c0 = v4 == 0 ? -1952 : (v4 == 1 ? -1056 : (v4 == 2 ? -928 : -288));
    unsigned aad = cb + (unsigned)(kb + 16 * (g - ((r + 3) >> 2)) + 2 * KZ_ + 2 * c0 - 192);
    auto boff = [&](int c) { const int c8 = c >> 3; return 16 * (c8 + 2 * (c8 >> 4) + 4); };
#define CV_B(dst, c, T) do { int a_ = ulv + boff(c) + 2304 * (T); a_ = a_ < ubv ? ubv : a_; DS_B128(dst.u, a_, 0); } while (0)
#define MF16(acc_, A_, B_) asm volatile("v_mfma_f32_16x16x32_f16 %0, %1, %2, %0" : "+v"(acc_) : "v"(A_.u), "v"(B_.u))
    f32x4 a0[4] = {}, a1[4] = {};
    if (v4 < 3) {
      const int T = (v4 == 2) ? 0 : 1;
      const int nit = v4 == 0 ? 7 : (v4 == 1 ? 6 : 5);
      Frag W0, W1, W2, W3, B0, B1, B2, B3;
      asm volatile("" : "+v"(a1[0]), "+v"(a1[1]), "+v"(a1[2]), "+v"(a1[3]));
      DS_B128(W1.u, aad, 0); DS_B128(W2.u, aad, 64); DS_B128(W3.u, aad, 128);
      CV_B(B0, c0, T); DS_B128(W0.u, aad, 192); CV_B(B1, c0 + 32, T);
      SCHED();
#define CV_STEP(F3, F2, F1, F0, BC, BN, k) \
      WAIT_LGKM(2); SCHED(); MF16(a1[3], F3, BC); DS_B128(F3.u, aad, 256 + 64 * (k)); CV_B(BN, c0 + 32 * (k) + 64, T); SCHED(); \
      MF16(a1[2], F2, BC); MF16(a1[1], F1, BC); WAIT_LGKM(3); SCHED(); MF16(a1[0], F0, BC); SCHED();
#pragma unroll 1
      for (int it = 0; it < nit; ++it) {
        CV_STEP(W1, W2, W3, W0, B0, B2, 0)
        CV_STEP(W2, W3, W0, W1, B1, B3, 1)
        CV_STEP(W3, W0, W1, W2, B2, B0, 2)
        CV_STEP(W0, W1, W2, W3, B3, B1, 3)
        c0 += 128; aad += 256;
        if (it == 1 && has_tile) {
          WAIT_VM(0);
          if (lane == 0) set_flag(hflag, ep);
        }
      }
      WAIT_LGKM(0);
      MFMA_DRAIN();
      SCHED();
    } else {
      Frag W0, W1, W2, W3, B0, B1, B2, B3;
      asm volatile("" : "+v"(a1[0]), "+v"(a1[1]), "+v"(a1[2]), "+v"(a1[3]), "+v"(a0[0]), "+v"(a0[1]), "+v"(a0[2]), "+v"(a0[3]));
      DS_B128(W1.u, aad, 0); DS_B128(W2.u, aad, 64); DS_B128(W3.u, aad, 128);
      CV_B(B0, c0, 0); CV_B(B2, c0, 1); DS_B128(W0.u, aad, 192);
      SCHED();
#define CV_STEP2(F3, F2, F1, F0, BC, CC, BN, CN, k) \
      WAIT_LGKM(1); SCHED(); MF16(a0[3], F3, BC); MF16(a1[3], F3, CC); \
      CV_B(BN, c0 + 32 * (k) + 32, 0); CV_B(CN, c0 + 32 * (k) + 32, 1); DS_B128(F3.u, aad, 256 + 64 * (k)); SCHED(); \
      MF16(a0[2], F2, BC); MF16(a1[2], F2, CC); MF16(a0[1], F1, BC); MF16(a1[1], F1, CC); \
      WAIT_LGKM(3); SCHED(); MF16(a0[0], F0, BC); MF16(a1[0], F0, CC); SCHED();
#pragma unroll 1
      for (int it = 0; it < 3; ++it) {
        CV_STEP2(W1, W2, W3, W0, B0, B2, B1, B3, 0)
        CV_STEP2(W2, W3, W0, W1, B1, B3, B0, B2, 1)
        CV_STEP2(W3, W0, W1, W2, B0, B2, B1, B3, 2)
        CV_STEP2(W0, W1, W2, W3, B1, B3, B0, B2, 3)
        c0 += 128; aad += 256;
        if (it == 0 && has_tile) {
          WAIT_VM(0);
          if (lane == 0) set_flag(hflag, ep);
        }
      }
      WAIT_LGKM(0); SCHED();
      MF16(a0[3], W1, B0); MF16(a1[3], W1, B2);
      MFMA_DRAIN();
      SCHED();
    }

    LAS float* xch = (LAS float*)(smem + CV_XCH + ch * 12288);
    auto put = [&](int slot, const f32x4& p0, const f32x4& p1) {
      *(LAS f32x4*)(xch + slot * 512 + lane * 4) = p0; *(LAS f32x4*)(xch + slot * 512 + 256 + lane * 4) = p1; };
    if (v4 == 0) put(0, a1[2], a1[3]);
    else if (v4 == 1) put(1, a1[0], a1[1]);
    else if (v4 == 2) put(4, a1[2], a1[3]);
    else { put(2, a1[0], a1[1]); put(3, a1[2], a1[3]); put(5, a0[0], a0[1]); }
    BARRIER();

    f32x4 y0, y1;
    {
      const int s1 = v4 == 0 ? 1 : (v4 == 1 ? 0 : (v4 == 2 ? 5 : 4));
      y0 = v4 == 3 ? a0[2] : ((v4 & 1) ? a1[2] : a1[0]);
      y1 = v4 == 3 ? a0[3] : ((v4 & 1) ? a1[3] : a1[1]);
      y0 += *(LAS f32x4*)(xch + s1 * 512 + lane * 4); y1 += *(LAS f32x4*)(xch + s1 * 512 + 256 + lane * 4);
      if (v4 < 2) { y0 += *(LAS f32x4*)(xch + (2 + v4) * 512 + lane * 4); y1 += *(LAS f32x4*)(xch + (2 + v4) * 512 + 256 + lane * 4); }
    }
    const float ki = *(LAS float*)cbase;
    const float dk = bf2f(A.dsk[d]);
    const int Tf = v4 < 2 ? 1 : 0, t0 = 2 * (v4 & 1);
    const int l1 = 8 * Tf + (r >> 1), sft = r & 1;
    const __amdgpu_buffer_rsrc_t rs = __builtin_amdgcn_make_buffer_rsrc(A.sT, 0, D_ * L_ * 2, 0x00020000);
#pragma unroll
    for (int tt = 0; tt < 2; ++tt) {
      const f32x4 y = tt ? y1 : y0;
      const float send0 = sft ? y[0] : y[2], send1 = sft ? y[1] : y[3];
      const float rcv0 = __builtin_bit_cast(float, __builtin_amdgcn_update_dpp(0, __builtin_bit_cast(int, send0), 0xB1, 0xf, 0xf, true));
      const float rcv1 = __builtin_bit_cast(float, __builtin_amdgcn_update_dpp(0, __builtin_bit_cast(int, send1), 0xB1, 0xf, 0xf, true));
      float z[4];
      z[0] = sft ? rcv0 : y[0]; z[1] = sft ? y[2] : rcv0; z[2] = sft ? rcv1 : y[1]; z[3] = sft ? y[3] : rcv1;
      const int l = 128 * l1 + 32 * (t0 + tt) + 8 * g + 4 * sft;
      const int c = l >> 3;
      const u32x2 uu = *(LAS u32x2*)(cbase + UB0 + 16 * (c + 2 * (c >> 4) + 4) + 8 * sft);
      float o[4];
#pragma unroll
      for (int i = 0; i < 4; ++i) {
        const unsigned hw = (i & 1) ? (uu[i >> 1] >> 16) : (uu[i >> 1] & 0xffffu);
        const float uf = (float)__builtin_bit_cast(_Float16, (u16)hw);
        o[i] = bfr(z[i] * ki) + bfr(uf * dk);
      }
      __builtin_amdgcn_raw_buffer_store_b64((u32x2){pkbf(o[0], o[1]), pkbf(o[2], o[3])}, rs, (unsigned)(d * L_ + l) * 2u, 0, 16);
    }

    BARRIER();
  }

  constexpr int NST = F_ / 64;
  u64* sflag = A.fl + FL_S + b * 8 + w;
  if (w >= 4) {
    const int j = w - 4;
    const char* abase = (const char*)(A.h + (size_t)g2m0 * HP_);
    unsigned aoff[2];
#pragma unroll
    for (int i = 0; i < 2; ++i) {
      const int r = 8 * (2 * j + i) + (lane >> 3);
      aoff[i] = (unsigned)(r * HP_ + 8 * ((lane & 7) ^ ((r >> 1) & 7))) * 2u;
    }
    const char* ap = abase;
    unsigned slot = 0;
#define G2_LDA() do { glds16_sc1(ap + aoff[0], smem + slot + (2 * j) * 1024); glds16_sc1(ap + aoff[1], smem + slot + (2 * j + 1) * 1024); \
                      ap += 128; slot = (slot == (G2_NA - 1) * 8192) ? 0u : slot + 8192; } while (0)
#pragma unroll 1
    for (int s = 0; s < 8; ++s) G2_LDA();
    WAIT_VM(16);
    if (lane == 0) set_flag(sflag, ep);
    WAIT_VM(15);
    BARRIER();
#pragma unroll 1
    for (int i = 0; i < 10; ++i) { WAIT_VM(10); BARRIER(); G2_LDA(); G2_LDA(); }
    WAIT_VM(10); BARRIER();
    WAIT_VM(6); BARRIER();
    WAIT_VM(2); BARRIER();
    WAIT_VM(0); BARRIER();
    return;
  }
  {
    const int wm = w >> 1, wn = w & 1;
    const int mrow = 32 * wm + (lane & 31), hh = lane >> 5;
    unsigned xoff[4];
#pragma unroll
    for (int ks = 0; ks < 4; ++ks) xoff[ks] = sbase + mrow * 128 + (((2 * ks + hh) ^ ((mrow >> 1) & 7)) << 4);
    const int g4 = (lane >> 4) & 1, q = (lane & 15) >> 2, p = lane & 3;
    const unsigned woff = sbase + G2_BR + (8 * hh + q) * 128 + (((2 * wn + (p & 1)) ^ (q & 2)) * 32) + 8 * (2 * g4 + (p >> 1));
    f32x16 acc = {};
    struct G2Frags { Frag x[4]; Frag w[4]; } FA, FB;
#define G2_RD(S, sa, sb) do { _Pragma("unroll") for (int ks = 0; ks < 4; ++ks) { \
      DS_B128(S.x[ks].u, xoff[ks] + (sa), 0); DS_TR(S.w[ks].s.a, woff + (sb), ks * 2048); DS_TR(S.w[ks].s.b, woff + (sb), ks * 2048 + 512); } } while (0)
#define G2_MM(S) do { _Pragma("unroll") for (int ks = 0; ks < 4; ++ks) MFMA32_BF16(acc, S.w[ks].u, S.x[ks].u); } while (0)
#define G2_RD1(S, ks, sa, sb) do { DS_B128(S.x[ks].u, xoff[ks] + (sa), 0); DS_TR(S.w[ks].s.a, woff + (sb), ks * 2048); DS_TR(S.w[ks].s.b, woff + (sb), ks * 2048 + 512); } while (0)
#define G2_BAR() do { asm volatile("" ::: "memory"); __builtin_amdgcn_s_barrier(); asm volatile("" ::: "memory"); SCHED(); } while (0)
#define G2_IT(CUR, NXT) do { \
    WAIT_LGKM(9); SCHED(); MFMA32_BF16(acc, CUR.w[0].u, CUR.x[0].u); G2_RD1(NXT, 0, an, bn); SCHED(); \
    WAIT_LGKM(9); SCHED(); MFMA32_BF16(acc, CUR.w[1].u, CUR.x[1].u); G2_RD1(NXT, 1, an, bn); SCHED(); \
    WAIT_LGKM(9); SCHED(); MFMA32_BF16(acc, CUR.w[2].u, CUR.x[2].u); G2_RD1(NXT, 2, an, bn); SCHED(); \
    WAIT_LGKM(9); SCHED(); MFMA32_BF16(acc, CUR.w[3].u, CUR.x[3].u); G2_RD1(NXT, 3, an, bn); SCHED(); \
    an = (an == (G2_NA - 1) * 8192) ? 0u : an + 8192; bn = (bn == (G2_NB - 1) * 8192) ? 0u : bn + 8192; } while (0)
    const size_t go = (size_t)(g2m0 + mrow) * D_ + g2n0 + 32 * wn + 16 * hh;
    u32x4 xa, xb;
    asm volatile("global_load_dwordx4 %0, %2, off\n\tglobal_load_dwordx4 %1, %2, off offset:16" : "=&v"(xa), "=&v"(xb) : "v"(A.x + go) : "memory");
    WAIT_VM(2);
    if (lane == 0) set_flag(sflag, ep);
    BARRIER(); SCHED();

    G2_RD(FA, 0u, 0u);
    SCHED();
    unsigned an = 8192, bn = 8192;
#pragma unroll 1
    for (int i = 0; i < 13; ++i) { G2_BAR(); G2_IT(FA, FB); G2_IT(FB, FA); }
    G2_BAR();
    G2_IT(FA, FB);
    WAIT_LGKM(0); SCHED();
    G2_MM(FB);
    MFMA_DRAIN();
    SCHED();

    asm volatile("s_waitcnt vmcnt(0)" : "+v"(xa), "+v"(xb) :: "memory");
    BARRIER();
    float o[16];
#pragma unroll
    for (int a = 0; a < 4; ++a) {
      const s16x4 sv = trread(smem + ST_OFF + (32 * wn + 16 * hh + 4 * a + q) * 144 + (32 * wm + 16 * g4 + 4 * p) * 2);
#pragma unroll
      for (int i = 0; i < 4; ++i) {
        const int e = 4 * a + i;
        const float ov = bfr(acc[e]);
        const float gv = bfr(ov * __builtin_amdgcn_rcpf(1.0f + __builtin_amdgcn_exp2f(-1.4426950408889634f * ov)));
        const float sf = bf2f((unsigned)(u16)sv[i]);
        const unsigned xw = e < 8 ? xa[e >> 1] : xb[(e - 8) >> 1];
        const float xf = (e & 1) ? bfhi(xw) : bflo(xw);
        o[e] = xf + bfr(sf * gv);
      }
    }
    u32x4 va, vb;
#pragma unroll
    for (int j = 0; j < 4; ++j) { va[j] = pkbf(o[2 * j], o[2 * j + 1]); vb[j] = pkbf(o[8 + 2 * j], o[9 + 2 * j]); }
    *(u32x4*)(A.out + go) = va;
    *(u32x4*)(A.out + go + 8) = vb;

  }
}

#if defined(__HIP_DEVICE_COMPILE__)
#pragma clang attribute pop
#endif

extern "C" void kernel_launch(void* const* d_in, const int* in_sizes, int n_in, void* d_out, int out_size,
                              void* d_ws, size_t ws_size, hipStream_t stream) {
  char* ws = (char*)d_ws;
  KFArgs a;
  a.x = (const u16*)d_in[0]; a.lAr = (const u16*)d_in[1]; a.Aim = (const u16*)d_in[2]; a.Cr = (const u16*)d_in[3];
  a.Ci = (const u16*)d_in[4]; a.ldt = (const u16*)d_in[5]; a.dsk = (const u16*)d_in[6];
  a.wg = (const u16*)d_in[8]; a.wu = (const u16*)d_in[9]; a.wd = (const u16*)d_in[10];
  a.uT = (u16*)(ws + OFF_UT);
  a.h = (u16*)(ws + OFF_H); a.sT = (u16*)(ws + OFF_ST); a.fl = (u64*)(ws + OFF_FL); a.out = (u16*)d_out;
  (void)hipFuncSetAttribute((const void*)kf_main, hipFuncAttributeMaxDynamicSharedMemorySize, KF_LDS);
  kf_main<<<256, 1024, KF_LDS, stream>>>(a);
}
```

```cpp
#include <hip/hip_runtime.h>
#include <hip/hip_bf16.h>
#include <stdint.h>

#if defined(__HIP_DEVICE_COMPILE__)
#pragma clang attribute push(__attribute__((target("no-packed-fp32-ops"))), apply_to = function)
#endif

#define U2F(x) __builtin_bit_cast(float, (unsigned)(x))
#define F2U(x) __builtin_bit_cast(unsigned, (float)(x))
#define TIDX ((int)__builtin_amdgcn_workitem_id_x())
#define BIDX ((int)__builtin_amdgcn_workgroup_id_x())

typedef unsigned short u16;
typedef __attribute__((ext_vector_type(4))) short s16x4;
typedef __attribute__((ext_vector_type(8))) __bf16 bf16x8;
typedef __attribute__((ext_vector_type(8))) _Float16 f16x8;
typedef __attribute__((ext_vector_type(2))) _Float16 f16x2;
typedef __attribute__((ext_vector_type(2))) __fp16 h16x2;
typedef __attribute__((ext_vector_type(4))) float f32x4;
typedef __attribute__((ext_vector_type(2))) float f32x2;
typedef __attribute__((ext_vector_type(16))) float f32x16;
typedef __attribute__((ext_vector_type(4))) unsigned u32x4;
typedef __attribute__((ext_vector_type(2))) unsigned u32x2;
#define LAS __attribute__((address_space(3)))
#define GAS __attribute__((address_space(1)))

constexpr int L_ = 2048, D_ = 512, N_ = 128, F_ = 1792;
constexpr int HP_ = 1856;
constexpr int KZ_ = 2080;

constexpr size_t OFF_FL = 0;
constexpr size_t OFF_UT = 256u << 10;
constexpr size_t OFF_ST = OFF_UT + (2u << 20);
constexpr size_t OFF_H = OFF_ST + (2u << 20);

__device__ __forceinline__ float bf2f(unsigned v) { return U2F(v << 16); }
__device__ __forceinline__ float bflo(unsigned v) { return U2F(v << 16); }
__device__ __forceinline__ float bfhi(unsigned v) { return U2F(v & 0xffff0000u); }
__device__ __forceinline__ float bfr(float f) { return (float)(__bf16)f; }
__device__ __forceinline__ unsigned pkbf(float a, float b) {
  typedef __attribute__((ext_vector_type(2))) __bf16 bf16x2;
  bf16x2 v; v.x = (__bf16)a; v.y = (__bf16)b;
  return __builtin_bit_cast(unsigned, v);
}
__device__ __forceinline__ unsigned pkh(float a, float b) {
  f16x2 v; v.x = (_Float16)a; v.y = (_Float16)b;
  return __builtin_bit_cast(unsigned, v);
}
__device__ __forceinline__ void glds16(const void* g, LAS void* l) {
  __builtin_amdgcn_global_load_lds((const GAS void*)g, l, 16, 0, 0);
}
__device__ __forceinline__ void glds16_sc1(const void* g, LAS void* l) {
  __builtin_amdgcn_global_load_lds((const GAS void*)g, l, 16, 0, 16);
}
__device__ __forceinline__ s16x4 trread(LAS char* p) {
  return __builtin_amdgcn_ds_read_tr16_b64_v4i16((LAS s16x4*)p);
}
union Frag { struct { s16x4 a, b; } s; bf16x8 bf; f16x8 h; u32x4 u; };

#define WAIT_VM(n) asm volatile("s_waitcnt vmcnt(" #n ")" ::: "memory")
#define WAIT_VMN(n) asm volatile("s_waitcnt vmcnt(%0)" :: "i"(n) : "memory")
#define BARRIER() do { asm volatile("s_waitcnt lgkmcnt(0)" ::: "memory"); __builtin_amdgcn_s_barrier(); asm volatile("" ::: "memory"); } while (0)

#define DS_TR(dst, addr, off) asm volatile("ds_read_b64_tr_b16 %0, %1 offset:%2" : "=v"(dst) : "v"(addr), "i"(off))
#define DS_B128(dst, addr, off) asm volatile("ds_read_b128 %0, %1 offset:%2" : "=v"(dst) : "v"(addr), "i"(off))
#define WAIT_LGKM(n) asm volatile("s_waitcnt lgkmcnt(" #n ")" ::: "memory")
#define SCHED() __builtin_amdgcn_sched_barrier(0)
#define MFMA32_BF16(acc, a, b) asm volatile("v_mfma_f32_32x32x16_bf16 %0, %1, %2, %0" : "+v"(acc) : "v"(a), "v"(b))
#define MFMA_DRAIN() asm volatile("s_nop 15\n\ts_nop 7" ::: "memory")

__device__ __forceinline__ float wave_sum(float v) {
  v += __builtin_bit_cast(float, __builtin_amdgcn_update_dpp(0, __builtin_bit_cast(int, v), 0xB1, 0xf, 0xf, true));
  v += __builtin_bit_cast(float, __builtin_amdgcn_update_dpp(0, __builtin_bit_cast(int, v), 0x4E, 0xf, 0xf, true));
  v += __builtin_bit_cast(float, __builtin_amdgcn_update_dpp(0, __builtin_bit_cast(int, v), 0x141, 0xf, 0xf, true));
  v += __builtin_bit_cast(float, __builtin_amdgcn_update_dpp(0, __builtin_bit_cast(int, v), 0x140, 0xf, 0xf, true));
  const float r0 = __builtin_bit_cast(float, __builtin_amdgcn_readlane(__builtin_bit_cast(int, v), 0));
  const float r1 = __builtin_bit_cast(float, __builtin_amdgcn_readlane(__builtin_bit_cast(int, v), 16));
  const float r2 = __builtin_bit_cast(float, __builtin_amdgcn_readlane(__builtin_bit_cast(int, v), 32));
  const float r3 = __builtin_bit_cast(float, __builtin_amdgcn_readlane(__builtin_bit_cast(int, v), 48));
  return (r0 + r1) + (r2 + r3);
}
__device__ __forceinline__ void cexp_p(float arl2, float wh, float wl, float p, float& re, float& im) {
  const float mag = __builtin_amdgcn_exp2f(arl2 * p);
  const float fr = __builtin_fmaf(wl, p, __builtin_amdgcn_fractf(wh * p));
  re = mag * __builtin_amdgcn_cosf(fr);
  im = mag * __builtin_amdgcn_sinf(fr);
}
__device__ __forceinline__ void split_h(float a, float b, unsigned& hi, unsigned& lo) {
  h16x2 h = __builtin_amdgcn_cvt_pkrtz(a, b);
  float ra = a - (float)h.x, rb = b - (float)h.y;
  h16x2 l = __builtin_amdgcn_cvt_pkrtz(ra, rb);
  hi = __builtin_bit_cast(unsigned, h);
  lo = __builtin_bit_cast(unsigned, l);
}

constexpr int G1_A = 16384, G1_W = 32768, G1_STAGE = G1_A + G1_W;
constexpr int KF_LDS = 163840;
constexpr int HS_PART = 98304;
constexpr int HS_CS = 147456;
constexpr int HS_ST = HS_CS + 1024;
constexpr int HS_PRM = 158720;
constexpr int CV_KS = 4672, CV_US = 4736, CV_CH = 4 * CV_KS + 2 * CV_US;
constexpr int KB2 = 64, KB4 = 64 + CV_KS, KB6 = 64 + 2 * CV_KS, KB0 = 64 + 3 * CV_KS - 16;
constexpr int UB0 = 4 * CV_KS + 64, UB1 = UB0 + CV_US;
constexpr int CV_XCH = 57344;
constexpr int G2_BR = 81920, G2_NB = 10, G2_NA = 10;
constexpr int ST_OFF = 2 * 8192;
constexpr int FL_LN = 0, FL_H = 2048, FL_S = 4096, FL_U = 6144, FL_CS = 8192;

__device__ __forceinline__ float gelu_t(float x) {
  const float c1 = -2.0f * 0.7978845608028654f * 1.4426950408889634f;
  const float c2 = c1 * 0.044715f;
  float a = x * __builtin_fmaf(x * x, c2, c1);
  float e = __builtin_amdgcn_exp2f(a);
  return x * __builtin_amdgcn_rcpf(1.0f + e);
}
typedef unsigned long long u64;
extern "C" __device__ u64 kf_dispatch_id() __asm("llvm.amdgcn.dispatch.id");
__device__ __forceinline__ u64 launch_epoch() {
  const u64 x = kf_dispatch_id() * 0x9E3779B97F4A7C15ull + (u64)(size_t)__builtin_amdgcn_dispatch_ptr() * 0xC2B2AE3D27D4EB4Full;
  return (x ^ (x >> 29)) | 0x8000000000000001ull;
}
__device__ __forceinline__ void set_flag(u64* p, u64 e) { __hip_atomic_store((GAS u64*)p, e, __ATOMIC_RELAXED, __HIP_MEMORY_SCOPE_AGENT); }
__device__ __forceinline__ void put_gran(u64* p, unsigned v, u64 e) { set_flag(p, (e & 0xffffffff00000000ull) | v); }
__device__ __forceinline__ unsigned get_gran(const u64* p, u64 g, u64 e) {
  for (unsigned spins = 0; spins < (1u << 20) && __builtin_amdgcn_ballot_w64((g >> 32) != (e >> 32)) != 0ull; ++spins) {
    __builtin_amdgcn_s_sleep(1);
    g = __hip_atomic_load((GAS u64*)p, __ATOMIC_RELAXED, __HIP_MEMORY_SCOPE_AGENT);
  }
  return (unsigned)g;
}
__device__ __forceinline__ void wait_flags2(const u64* p0, const u64* p1, u64 e) {
  for (unsigned spins = 0; spins < (1u << 20); ++spins) {
    const u64 a = __hip_atomic_load((GAS u64*)p0, __ATOMIC_RELAXED, __HIP_MEMORY_SCOPE_AGENT);
    const u64 b = __hip_atomic_load((GAS u64*)p1, __ATOMIC_RELAXED, __HIP_MEMORY_SCOPE_AGENT);
    if (__builtin_amdgcn_ballot_w64(a != e || b != e) == 0ull) break;
    __builtin_amdgcn_s_sleep(1);
  }
}

struct KIdx { u16 v[2304]; };
constexpr KIdx make_kidx() {
  KIdx t{};
  for (int x = 0; x < 2304; ++x) {
    const int idx = KZ_ - x;
    int o = 21 * 32 + 1;
    if (idx >= 0 && idx < L_) {
      int p = idx;
      int sh = idx >= 1024 ? 3 : (idx >= 512 ? 2 : (idx >= 256 ? 1 : 0));
      if (sh) {
        const int q = idx >> sh, rem = idx & ((1 << sh) - 1), half = 1 << (sh - 1);
        p = (q + ((rem > half || (rem == half && (q & 1))) ? 1 : 0)) << sh;
      }
      const int r = p & 7, cc = p >> 3, c1 = cc >> 5, c0 = cc & 31;
      const int rw = c1 == 0 ? r : (c1 == 1 ? 8 + (r >> 1) : (c1 < 4 ? 8 + 2 * c1 + (r >> 2) : 12 + c1));
      o = rw * 32 + c0;
    }
    t.v[x] = (u16)(o * 4);
  }
  return t;
}
__device__ const KIdx g_kidx = make_kidx();

struct G1Frags { Frag x[2]; Frag w[2]; };
struct KFArgs {
  const u16 *x, *lAr, *Aim, *Cr, *Ci, *ldt, *dsk, *wg, *wu, *wd;
  u16 *uT, *h, *sT; u64* fl; u16* out;
};

#define HBAR() do { SCHED(); asm volatile("" ::: "memory"); __builtin_amdgcn_s_barrier(); asm volatile("" ::: "memory"); SCHED(); } while (0)
__device__ __forceinline__ void kf_helper(LAS char* smem, const KFArgs& A, int j, int lane, int g2m0, int g2n0, u64 ep, bool has_tile, int tmt, int tn0, int tkb) {
  const int b = BIDX, ch = j >> 2, nq = j & 3, d = 2 * b + ch;
  LAS float* prm = (LAS float*)(smem + HS_PRM) + j * 160;
  LAS float* part = (LAS float*)(smem + HS_PART) + (j >> 1) * 704;
  const int uc = 64 * nq + lane;
  unsigned warm = 0, warm2 = 0;
  const int ht = j * 64 + lane, bi = b >> 3;
#define TOUCH(reg, ptr) asm volatile("global_load_dword %0, %1, off" : "+v"(reg) : "v"(ptr) : "memory")
  const u32x4 kx0 = *(const u32x4*)(g_kidx.v + 8 * (4 + uc));
  const int c2 = lane < 4 ? lane : 256 + (lane < 21 ? lane : 20);
  const u32x4 kx1 = *(const u32x4*)(g_kidx.v + 8 * c2);
  float kinv;
  u32x2 xt[4] = {};
  {
    const int n = d * N_ + nq * 32 + (lane & 31);
    const u32x4 xv = *(const u32x4*)(A.x + (size_t)(b * 8 + j) * D_ + lane * 8);
    const unsigned vdt = A.ldt[d];
    const unsigned a = *(const unsigned*)(A.Cr + d * N_ + 2 * lane), bb = *(const unsigned*)(A.Ci + d * N_ + 2 * lane);
    const unsigned v0 = A.lAr[n], v1 = A.Aim[n], v2 = A.Cr[n], v3 = A.Ci[n];
    HBAR();
    if (tkb >= 0) {
      const int q = (lane & 15) >> 2, p = lane & 3, chk = 2 * (j & 3) + (p >> 1);
#pragma unroll
      for (int t = 0; t < 4; ++t) {
        const int ra = 64 * (j >> 2) + 32 * (t >> 1) + 8 * (lane >> 4) + 4 * (t & 1) + q;
        xt[t] = __builtin_bit_cast(u32x2, trread(smem + ra * 128 + ((chk ^ ((ra >> 1) & 7)) << 4) + 8 * (p & 1)));
      }
    }
    {
      float f[8];
#pragma unroll
      for (int e = 0; e < 4; ++e) { f[2 * e] = bflo(xv[e]); f[2 * e + 1] = bfhi(xv[e]); }
      const float mean = wave_sum(((f[0] + f[1]) + (f[2] + f[3])) + ((f[4] + f[5]) + (f[6] + f[7]))) * (1.0f / D_);
      float q2 = 0.f;
#pragma unroll
      for (int e = 0; e < 8; ++e) { const float dd = f[e] - mean; q2 = __builtin_fmaf(dd, dd, q2); }
      const float var = wave_sum(q2) * (1.0f / D_);
      const float sd = __builtin_sqrtf(bfr(bfr(var) + 1e-5f));
      if (lane == 0) put_gran(A.fl + FL_LN + b * 8 + j, pkbf(mean, sd), ep);
    }
    const float dt = bfr(__builtin_expf(bf2f(vdt)));
    const float sb = wave_sum((__builtin_fabsf(bflo(a)) + __builtin_fabsf(bfhi(a))) + (__builtin_fabsf(bflo(bb)) + __builtin_fabsf(bfhi(bb)))) * dt * 2.0f;
    const int ex = (int)((F2U(sb) >> 23) & 0xff) - 127;
    int se = 14 - ex;
    se = se > 100 ? 100 : (se < -100 ? -100 : se);
    const float cs = dt * 2.0f * U2F((unsigned)(127 + se) << 23);
    kinv = U2F((unsigned)(127 - se) << 23);
    const float ar = -bfr(__builtin_expf(bf2f(v0))) * dt;
    const float ai = bf2f(v1) * dt;
    const float w0 = ai * 0.15915494309189535f;
    const float wh = U2F(F2U(w0) & 0xfffff000u);
    const float wl = __builtin_fmaf(ai, 6.4206383e-09f, __builtin_fmaf(ai, 0.15915494309189535f, -wh));
    if (lane < 32) {
      prm[lane] = ar * 1.4426950408889634f; prm[32 + lane] = wh; prm[64 + lane] = wl;
      prm[96 + lane] = bf2f(v2) * cs; prm[128 + lane] = bf2f(v3) * cs;
    }
  }
  const int row = lane & 31, hh = lane >> 5;
  int prow;
  if (row < 8) prow = row;
  else if (row < 12) prow = 256 + 2 * (row - 8);
  else if (row < 16) prow = 512 + 256 * ((row - 12) >> 1) + 4 * ((row - 12) & 1);
  else prow = 1024 + 256 * (row - 16);
  prow = row < 21 ? prow : 0;
  const float pA = (float)prow, pB = (float)(8 * row);
  f32x16 acc = {};
  const int pnl = 4 * nq + ((lane & 7) < 4 ? (lane & 7) : 4);
  const u64* f0 = A.fl + FL_U + (pnl < 16 ? pnl : 15) * 64 + (d >> 6) * 8 + ((d >> 4) & 3) + 4 * ((lane >> 3) & 1);
  const u64* gp = j < 2 ? A.fl + FL_LN + tmt * 128 + ht : A.fl + FL_CS + ((j >> 1) & 1) * F_ + tn0 + (j & 1) * 64 + lane;
  u64 fa = 0, gr = 0;
  typedef __attribute__((ext_vector_type(4))) _Float16 f16x4;
#pragma unroll 1
  for (int hs = 0; hs < 8; ++hs) {
    if (hs == 3 || hs == 5 || hs == 7) {
      asm volatile("s_waitcnt vmcnt(0)" : "+v"(gr) :: "memory");
      if (hs == 3 && has_tile && j < 2) {
        const unsigned g = get_gran(gp, gr, ep);
        *(LAS f32x2*)(smem + HS_ST + ht * 8) = (f32x2){bflo(g), __builtin_amdgcn_rcpf(bfhi(g))};
      }
      if (hs == 5 && tkb >= 0 && lane == 0) set_flag(A.fl + FL_U + (tmt * 8 + tkb) * 8 + j, ep);
      if (hs == 7) {
        if (has_tile && j >= 4) *(LAS float*)(smem + HS_CS + (ht - 256) * 4) = U2F(get_gran(gp, gr, ep));
        asm volatile("global_load_dwordx2 %0, %1, off sc1" : "+v"(fa) : "v"(f0) : "memory");
      }
    }

    BARRIER();
    if (has_tile && ((hs == 1 && j < 2) || (hs == 5 && j >= 4)))
      asm volatile("global_load_dwordx2 %0, %1, off sc1" : "+v"(gr) : "v"(gp) : "memory");
    if (hs == 3 && tkb >= 0) {
      const __amdgpu_buffer_rsrc_t ru = __builtin_amdgcn_make_buffer_rsrc(A.uT, 0, L_ * D_ * 2, 0x00020000);
#pragma unroll
      for (int t = 0; t < 2; ++t) {
        const int r0 = 64 * (j >> 2) + 32 * t + 8 * (lane >> 4);
        u32x4 o;
#pragma unroll
        for (int e = 0; e < 4; ++e) {
          const f32x4 st = *(const LAS f32x4*)(smem + HS_ST + (r0 + 2 * e) * 8);
          const unsigned xw = xt[2 * t + (e >> 1)][e & 1];
          const unsigned tt = pkbf(bflo(xw) - st[0], bfhi(xw) - st[2]);
          const unsigned oo = pkbf(bflo(tt) * st[1], bfhi(tt) * st[3]);
          o[e] = __builtin_bit_cast(unsigned, __builtin_amdgcn_cvt_pkrtz(bflo(oo), bfhi(oo)));
        }
        __builtin_amdgcn_raw_buffer_store_b128(o, ru, (unsigned)((64 * tkb + 16 * (j & 3) + (lane & 15)) * L_ + 128 * tmt + r0) * 2u, 0, 16);
      }
    }
    const int n0 = hs * 4 + hh * 2;
    const f32x2 va = *(LAS f32x2*)(prm + n0), vh = *(LAS f32x2*)(prm + 32 + n0), vl = *(LAS f32x2*)(prm + 64 + n0);
    const f32x2 vr = *(LAS f32x2*)(prm + 96 + n0), vi = *(LAS f32x2*)(prm + 128 + n0);
    u32x2 Ah, Al, Bh, Bl;
#pragma unroll
    for (int q = 0; q < 2; ++q) {
      float er, ei, yr, yi;
      cexp_p(va[q], vh[q], vl[q], pA, er, ei);
      cexp_p(va[q], vh[q], vl[q], pB, yr, yi);
      const float xr = vr[q] * er - vi[q] * ei, xi = vr[q] * ei + vi[q] * er;
      unsigned h0, l0, h1, l1;
      split_h(xr, xi, h0, l0);
      split_h(yr, -yi, h1, l1);
      Ah[q] = h0; Al[q] = l0; Bh[q] = h1; Bl[q] = l1;
    }
    acc = __builtin_amdgcn_mfma_f32_32x32x8f16(__builtin_bit_cast(f16x4, Al), __builtin_bit_cast(f16x4, Bh), acc, 0, 0, 0);
    acc = __builtin_amdgcn_mfma_f32_32x32x8f16(__builtin_bit_cast(f16x4, Ah), __builtin_bit_cast(f16x4, Bl), acc, 0, 0, 0);
    acc = __builtin_amdgcn_mfma_f32_32x32x8f16(__builtin_bit_cast(f16x4, Ah), __builtin_bit_cast(f16x4, Bh), acc, 0, 0, 0);
  }
  if ((j & 1) == 0) {
#pragma unroll
    for (int i = 0; i < 12; ++i) {
      const int rr = (i & 3) + 8 * (i >> 2) + 4 * hh;
      if (rr < 21) part[rr * 32 + row] = acc[i];
    }
    if (lane == 0) part[21 * 32 + 1] = 0.f;
  }
  BARRIER();
  asm volatile("s_waitcnt vmcnt(0)" : "+v"(fa) :: "memory");
  if (__builtin_amdgcn_ballot_w64(fa != ep) != 0ull) wait_flags2(f0, f0, ep);
  u32x4 ua = {0u, 0u, 0u, 0u}; unsigned unx = 0;
  {
    const u16* ur = A.uT + (size_t)d * L_ + 8 * uc;
    asm volatile("global_load_dwordx4 %0, %2, off sc1\n\tglobal_load_ushort %1, %3, off sc1" : "+v"(ua), "+v"(unx) : "v"(ur), "v"(ur + (uc < 255 ? 8 : 0)) : "memory");
  }
  if (j & 1) {
#pragma unroll
    for (int i = 0; i < 12; ++i) {
      const int rr = (i & 3) + 8 * (i >> 2) + 4 * hh;
      if (rr < 21) part[rr * 32 + row] += acc[i];
    }
  }
  BARRIER();
  {
    const LAS char* t0 = smem + HS_PART + (2 * ch) * 2816;
    LAS char* cbase = smem + ch * CV_CH;
    auto expand = [&](const u32x4& kx, int c) {
      float v[8];
#pragma unroll
      for (int e = 0; e < 8; ++e) {
        const unsigned o = (e & 1) ? (kx[e >> 1] >> 16) : (kx[e >> 1] & 0xffffu);
        v[e] = *(const LAS float*)(t0 + o) + *(const LAS float*)(t0 + o + 2816);
      }
      u32x4 o;
#pragma unroll
      for (int e = 0; e < 4; ++e) o[e] = pkh(v[2 * e], v[2 * e + 1]);
      *(LAS u32x4*)(cbase + KB0 + 16 * c) = o;
      LAS unsigned* q2 = (LAS unsigned*)(cbase + KB2 + 16 * c - 4);
      LAS u32x2* q4 = (LAS u32x2*)(cbase + KB4 + 16 * c - 8);
      LAS unsigned* q6 = (LAS unsigned*)(cbase + KB6 + 16 * c - 12);
#pragma unroll
      for (int e = 0; e < 4; ++e) { q2[e] = o[e]; q6[e] = o[e]; }
      q4[0] = (u32x2){o[0], o[1]}; q4[1] = (u32x2){o[2], o[3]};
    };
    expand(kx0, 4 + uc);
    if (nq == 0) {
      if (lane < 21) expand(kx1, c2);
      if (lane == 0) *(LAS float*)(cbase) = kinv;
    }
    asm volatile("s_waitcnt vmcnt(0)" : "+v"(ua), "+v"(unx) :: "memory");
    const unsigned nx = (uc < 255) ? unx : 0u;
    const int P = uc + 2 * (uc >> 4) + 4;
    *(LAS u32x4*)(cbase + UB0 + 16 * P) = ua;
    u32x4 s1;
    s1[0] = (ua[0] >> 16) | (ua[1] << 16); s1[1] = (ua[1] >> 16) | (ua[2] << 16);
    s1[2] = (ua[2] >> 16) | (ua[3] << 16); s1[3] = (ua[3] >> 16) | (nx << 16);
    *(LAS u32x4*)(cbase + UB1 + 16 * P) = s1;
    if (uc == 0) {
      const u32x4 z = {0u, 0u, 0u, 0u};
      *(LAS u32x4*)(cbase + UB0) = z; *(LAS u32x4*)(cbase + UB0 + 16) = z;
      *(LAS u32x4*)(cbase + UB1) = z; *(LAS u32x4*)(cbase + UB1 + 16) = (u32x4){0u, 0u, 0u, ua[0] << 16};
    }
  }

  BARRIER();

  {
    constexpr int NST = F_ / 64;
    const char* bbase = (const char*)(A.wd + g2n0);
    const int r = 8 * j + (lane >> 3);
    const unsigned boff = (unsigned)(r * D_) * 2u + (unsigned)((((lane & 7) >> 1) ^ (r & 2)) * 32 + (lane & 1) * 16);
    const char* bp = bbase + boff;
    unsigned slot = 0;
    TOUCH(warm, (const char*)A.wd + (size_t)(bi * 448 + (ht < 448 ? ht : 447)) * 128);
#pragma unroll 1
    for (int s = 0; s < 8; ++s) { glds16(bp, smem + G2_BR + slot + j * 1024); bp += 128 * D_; slot += 8192; }
    BARRIER();
    if (j == 0) { const u64* hf = A.fl + FL_H + (g2m0 >> 7) * 128; wait_flags2(hf + lane, hf + (lane < 48 ? 64 + lane : lane), ep); }
    BARRIER();
    {
      const int t = (g2n0 >> 6) * 224 + (ht < 224 ? ht : 223);
      const int ln = t >> 6, r = t & 63;
      TOUCH(warm2, (const char*)A.h + (size_t)(g2m0 + r) * (HP_ * 2) + ln * 128);
    }
    asm volatile("s_waitcnt vmcnt(8)" : "+v"(warm) :: "memory");
    BARRIER();
    const u64* sf = A.fl + FL_S + (g2n0 >> 6) * 256 + 32 * j + (lane & 31);
    u64 sflag = 0;
    u32x4 sv = {0u, 0u, 0u, 0u};
    const int sc = j * 64 + lane;
#pragma unroll 1
    for (int i = 0; i < 10; ++i) {
      WAIT_VM(5);
      BARRIER();
      if (i == 6) asm volatile("global_load_dwordx2 %0, %1, off sc1" : "+v"(sflag) : "v"(sf) : "memory");
      glds16(bp, smem + G2_BR + slot + j * 1024);
      glds16(bp + 128 * D_, smem + G2_BR + slot + 8192 + j * 1024);
      bp += 256 * D_;
      slot = (slot == 8 * 8192) ? 0u : slot + 16384;
    }
    asm volatile("s_waitcnt vmcnt(5)" : "+v"(sflag) :: "memory");
    BARRIER();
    {
      if (__builtin_amdgcn_ballot_w64(sflag != ep) != 0ull) wait_flags2(sf, sf, ep);
      const __amdgpu_buffer_rsrc_t rs = __builtin_amdgcn_make_buffer_rsrc(A.sT, 0, D_ * L_ * 2, 0x00020000);
      const unsigned so = (unsigned)(((g2n0 + (sc >> 3)) * L_ + g2m0 + 8 * (sc & 7)) * 2);
      asm volatile("buffer_load_dwordx4 %0, %1, %2, 0 offen sc1" : "+v"(sv) : "v"(so), "s"(rs) : "memory");
    }
    WAIT_VM(4); BARRIER();
    WAIT_VM(2); BARRIER();
    WAIT_VM(1); BARRIER();
    asm volatile("s_waitcnt vmcnt(0)" : "+v"(sv), "+v"(warm2) :: "memory");
    *(LAS u32x4*)(smem + ST_OFF + (sc >> 3) * 144 + (sc & 7) * 16) = sv;
    BARRIER();
  }
}

__global__ __launch_bounds__(1024, 4) void kf_main(KFArgs A) {
  extern __shared__ __attribute__((aligned(16))) char smem_[];
  LAS char* smem = (LAS char*)smem_;
  const unsigned sbase = (unsigned)(size_t)smem;
  const int tid = TIDX, lane = tid & 63;
  const int w = __builtin_amdgcn_readfirstlane(tid >> 6);
  const int b = BIDX, xcd = b & 7, idx = b >> 3;
  const bool has_tile = idx < 28;
  const int g2mt = xcd * 4 + (idx & 3), g2nt = idx >> 2;
  const int g2m0 = g2mt * 64, g2n0 = g2nt * 64;

  const u64 ep = launch_epoch();
  const int mt = (xcd & 3) * 4 + (idx & 3), nt = (xcd >> 2) * 7 + (has_tile ? idx >> 2 : 6);
  const int rot = nt & 7;
  if (w >= 8) { kf_helper(smem, A, w - 8, lane, g2m0, g2n0, ep, has_tile, mt, nt * 128, (has_tile && nt < 8) ? (nt & 7) : -1); return; }

  const int m0 = mt * 128, n0 = nt * 128;
  u64* hflag = A.fl + FL_H + mt * 128 + nt * 8 + w;
  const char* abase = (const char*)(A.x + (size_t)m0 * D_);
  const char* gbase = (const char*)(A.wg + n0);
  const char* ubase = (const char*)(A.wu + n0);
  const int arow = 16 * w + (lane >> 3);
  const unsigned aoff0 = (unsigned)(arow * D_ + 8 * ((lane & 7) ^ ((arow >> 1) & 7))) * 2u;
  const unsigned aoff1 = (unsigned)((arow + 8) * D_ + 8 * ((lane & 7) ^ (((arow + 8) >> 1) & 7))) * 2u;
  const int wk = 8 * w + (lane >> 3);
  const int fk = (wk >> 1) & 1;
  const unsigned woffg = (unsigned)(wk * F_) * 2u + (unsigned)((((lane & 7) >> 1) ^ fk) * 32 + (lane & 1) * 16);
  const unsigned woffu = (unsigned)(wk * F_) * 2u + (unsigned)((((lane & 7) >> 1) ^ (fk | 2)) * 32 + (lane & 1) * 16);
#define G1_LD(i, s_, sb) do { const int s = ((s_) + rot) & 7; \
    if ((i) == 0) glds16(abase + (s) * 128 + aoff0, smem + (sb) + (2 * w) * 1024); \
    if ((i) == 1) glds16(gbase + (long)(s) * (128 * F_) + woffg, smem + (sb) + G1_A + w * 1024); \
    if ((i) == 2) glds16(gbase + (long)(s) * (128 * F_) + 128 + woffg, smem + (sb) + G1_A + 8192 + w * 1024); \
    if ((i) == 3) glds16(abase + (s) * 128 + aoff1, smem + (sb) + (2 * w + 1) * 1024); \
    if ((i) == 4) glds16(ubase + (long)(s) * (128 * F_) + woffu, smem + (sb) + G1_A + 16384 + w * 1024); \
    if ((i) == 5) glds16(ubase + (long)(s) * (128 * F_) + 128 + woffu, smem + (sb) + G1_A + 24576 + w * 1024); } while (0)
  if (has_tile) {
#pragma unroll
    for (int i = 0; i < 6; ++i) G1_LD(i, 0, 0);
#pragma unroll
    for (int i = 0; i < 6; ++i) G1_LD(i, 1, G1_STAGE);
    const int wm = w & 1, wn = w >> 1;
    const int mrow = 64 * wm + (lane & 31), hh = lane >> 5;
    unsigned xoff[4];
#pragma unroll
    for (int ks = 0; ks < 4; ++ks) xoff[ks] = sbase + mrow * 128 + (((2 * ks + hh) ^ ((mrow >> 1) & 7)) << 4);
    const int mat = (lane >> 4) & 1, q = (lane & 15) >> 2, p = lane & 3;
    const int pp = ((p & 1) << 1) | (p >> 1);
    const int fq = ((q >> 1) & 1) | (mat << 1);
    const unsigned wrow = sbase + G1_A + (mat * 2 + (wn >> 1)) * 8192 + (8 * hh + q) * 128 + pp * 8;
    const unsigned woff0 = wrow + (((2 * (wn & 1)) ^ fq) * 32);
    const unsigned woff1 = wrow + (((2 * (wn & 1) + 1) ^ fq) * 32);
    f32x16 acc[2][2];
#pragma unroll
    for (int t = 0; t < 2; ++t) { acc[t][0] = (f32x16){}; acc[t][1] = (f32x16){}; }
    G1Frags FA, FB;
#define RD_W(S, t, b0, b1, ks) do { DS_TR(S.w[t].s.a, (t) ? b1 : b0, (ks) * 2048); DS_TR(S.w[t].s.b, (t) ? b1 : b0, (ks) * 2048 + 512); } while (0)
#define RD_X(S, XA) do { DS_B128(S.x[0].u, XA, 0); DS_B128(S.x[1].u, XA, 4096); } while (0)
#define MM1(S, t, m) MFMA32_BF16(acc[t][m], S.w[t].u, S.x[m].u)
    WAIT_VM(6);
    BARRIER();

    RD_X(FA, xoff[0]); RD_W(FA, 0, woff0, woff1, 0); RD_W(FA, 1, woff0, woff1, 0);
    SCHED();
    unsigned so = 0, sn = G1_STAGE, sp = 2 * G1_STAGE;
#define KSTEP(CUR, NXT, XA, KSN, L0, MORE) \
    WAIT_LGKM(2); SCHED(); MM1(CUR, 0, 0); RD_X(NXT, XA); SCHED(); \
    MM1(CUR, 0, 1); RD_W(NXT, 0, w0, w1, KSN); if (MORE) G1_LD(L0, st + 2, sp); SCHED(); \
    WAIT_LGKM(4); SCHED(); MM1(CUR, 1, 0); RD_W(NXT, 1, w0, w1, KSN); SCHED(); \
    MM1(CUR, 1, 1); if (MORE) G1_LD(L0 + 1, st + 2, sp); SCHED();
#define G1_BODY(MORE) { \
    const unsigned w0 = woff0 + so, w1 = woff1 + so; \
    const unsigned x1 = xoff[1] + so, x2 = xoff[2] + so, x3 = xoff[3] + so; \
    const unsigned xn0 = xoff[0] + sn, w0n = woff0 + sn, w1n = woff1 + sn; \
    KSTEP(FA, FB, x1, 1, 0, MORE) \
    KSTEP(FB, FA, x2, 2, 2, MORE) \
    KSTEP(FA, FB, x3, 3, 4, MORE) \
    WAIT_LGKM(2); SCHED(); MM1(FB, 0, 0); SCHED(); \
    if (MORE) WAIT_VM(6); else WAIT_VM(0); \
    BARRIER(); \
    SCHED(); \
    RD_X(FA, xn0); \
    MM1(FB, 0, 1); RD_W(FA, 0, w0n, w1n, 0); SCHED(); \
    MM1(FB, 1, 0); RD_W(FA, 1, w0n, w1n, 0); SCHED(); \
    MM1(FB, 1, 1); SCHED(); \
    sp = so; so = sn; sn = (sn == 2 * G1_STAGE) ? 0u : sn + G1_STAGE; }
#pragma unroll 1
    for (int st = 0; st < 6; ++st) G1_BODY(true)
#pragma unroll 1
    for (int st = 6; st < 8; ++st) G1_BODY(false)
    WAIT_LGKM(0);
    MFMA_DRAIN();
    SCHED();

    __builtin_amdgcn_s_setprio(2);
    const __amdgpu_buffer_rsrc_t rh = __builtin_amdgcn_make_buffer_rsrc(A.h, 0, L_ * HP_ * 2, 0x00020000);
    const unsigned ho = (unsigned)((m0 + mrow) * HP_ + n0 + 32 * wn + 8 * hh) * 2u;
#pragma unroll
    for (int t = 0; t < 2; ++t) {
      const LAS f32x4* cp = (const LAS f32x4*)(smem + HS_CS + (32 * wn + 16 * t + 8 * hh) * 4);
      const f32x4 cg0 = cp[0], cg1 = cp[1], cu0 = cp[32], cu1 = cp[33];
#pragma unroll
      for (int m = 0; m < 2; ++m) {
        const f32x2 st = *(const LAS f32x2*)(smem + HS_ST + (mrow + 32 * m) * 8);
        const float nm = -st[0] * st[1];
        u32x4 v;
#pragma unroll
        for (int j = 0; j < 4; ++j) {
          float o[2];
#pragma unroll
          for (int e = 0; e < 2; ++e) {
            const int i = 2 * j + e;
            const float g = __builtin_fmaf(acc[t][m][i], st[1], nm * (i < 4 ? cg0[i & 3] : cg1[i & 3]));
            const float u = __builtin_fmaf(acc[t][m][8 + i], st[1], nm * (i < 4 ? cu0[i & 3] : cu1[i & 3]));
            o[e] = gelu_t(g) * u;
          }
          v[j] = pkbf(o[0], o[1]);
        }
        __builtin_amdgcn_raw_buffer_store_b128(v, rh, ho + (unsigned)(m * 32 * HP_ + 16 * t) * 2u, 0, 16);
        if (t == 0) { SCHED(); BARRIER(); SCHED(); }
      }
    }
    __builtin_amdgcn_s_setprio(0);

    BARRIER();
  } else {
    const int q = b - 224;
    const u16* wsrc = (q < 16 ? A.wg : A.wu) + (q & 15) * 112;
    const int c = lane & 15, rs4 = lane >> 4;
    float sum[8] = {};
#pragma unroll
    for (int i = 0; i < 16; ++i) {
      const u32x4 v = *(const u32x4*)(wsrc + (size_t)(64 * w + 4 * i + rs4) * F_ + 8 * (c < 14 ? c : 13));
#pragma unroll
      for (int e = 0; e < 4; ++e) { sum[2 * e] += bflo(v[e]); sum[2 * e + 1] += bfhi(v[e]); }
    }
    LAS float* red = (LAS float*)smem;
#pragma unroll
    for (int e = 0; e < 8; ++e) red[(w * 4 + rs4) * 128 + c * 8 + e] = sum[e];
    BARRIER();
    if (tid < 112) {
      float t = 0.f;
#pragma unroll 8
      for (int i = 0; i < 32; ++i) t += red[i * 128 + tid];
      put_gran(A.fl + FL_CS + (q >> 4) * F_ + (q & 15) * 112 + tid, F2U(t), ep);
    }
#pragma unroll 1
    for (int i = 0; i < 11; ++i) BARRIER();
  }

  {
    const int ch = w >> 2, v4 = (w + 2 * ch) & 3, d = 2 * b + ch;
    LAS char* cbase = smem + ch * CV_CH;
    const unsigned cb = sbase + ch * CV_CH;
    const int r = lane & 15, g = lane >> 4;
    const int e = (-2 * r) & 7;
    const int kb = (e == 0) ? KB0 : (e == 2 ? KB2 : (e == 4 ? KB4 : KB6));
    const int ub = (r & 1) ? UB1 : UB0;
    const int ubv = (int)cb + ub;
    const int ulv = ubv + 16 * (18 * (r >> 1) + g);
    int c0 = v4 == 0 ? -1952 : (v4 == 1 ? -1056 : (v4 == 2 ? -928 : -288));
    unsigned aad = cb + (unsigned)(kb + 16 * (g - ((r + 3) >> 2)) + 2 * KZ_ + 2 * c0 - 192);
    auto boff = [&](int c) { const int c8 = c >> 3; return 16 * (c8 + 2 * (c8 >> 4) + 4); };
#define CV_B(dst, c, T) do { int a_ = ulv + boff(c) + 2304 * (T); a_ = a_ < ubv ? ubv : a_; DS_B128(dst.u, a_, 0); } while (0)
#define MF16(acc_, A_, B_) asm volatile("v_mfma_f32_16x16x32_f16 %0, %1, %2, %0" : "+v"(acc_) : "v"(A_.u), "v"(B_.u))
    f32x4 a0[4] = {}, a1[4] = {};
    if (v4 < 3) {
      const int T = (v4 == 2) ? 0 : 1;
      const int nit = v4 == 0 ? 7 : (v4 == 1 ? 6 : 5);
      Frag W0, W1, W2, W3, B0, B1, B2, B3;
      asm volatile("" : "+v"(a1[0]), "+v"(a1[1]), "+v"(a1[2]), "+v"(a1[3]));
      DS_B128(W1.u, aad, 0); DS_B128(W2.u, aad, 64); DS_B128(W3.u, aad, 128);
      CV_B(B0, c0, T); DS_B128(W0.u, aad, 192); CV_B(B1, c0 + 32, T);
      SCHED();
#define CV_STEP(F3, F2, F1, F0, BC, BN, k) \
      WAIT_LGKM(2); SCHED(); MF16(a1[3], F3, BC); DS_B128(F3.u, aad, 256 + 64 * (k)); CV_B(BN, c0 + 32 * (k) + 64, T); SCHED(); \
      MF16(a1[2], F2, BC); MF16(a1[1], F1, BC); WAIT_LGKM(3); SCHED(); MF16(a1[0], F0, BC); SCHED();
#pragma unroll 1
      for (int it = 0; it < nit; ++it) {
        CV_STEP(W1, W2, W3, W0, B0, B2, 0)
        CV_STEP(W2, W3, W0, W1, B1, B3, 1)
        CV_STEP(W3, W0, W1, W2, B2, B0, 2)
        CV_STEP(W0, W1, W2, W3, B3, B1, 3)
        c0 += 128; aad += 256;
        if (it == 1 && has_tile) {
          WAIT_VM(0);
          if (lane == 0) set_flag(hflag, ep);
        }
      }
      WAIT_LGKM(0);
      MFMA_DRAIN();
      SCHED();
    } else {
      Frag W0, W1, W2, W3, B0, B1, B2, B3;
      asm volatile("" : "+v"(a1[0]), "+v"(a1[1]), "+v"(a1[2]), "+v"(a1[3]), "+v"(a0[0]), "+v"(a0[1]), "+v"(a0[2]), "+v"(a0[3]));
      DS_B128(W1.u, aad, 0); DS_B128(W2.u, aad, 64); DS_B128(W3.u, aad, 128);
      CV_B(B0, c0, 0); CV_B(B2, c0, 1); DS_B128(W0.u, aad, 192);
      SCHED();
#define CV_STEP2(F3, F2, F1, F0, BC, CC, BN, CN, k) \
      WAIT_LGKM(1); SCHED(); MF16(a0[3], F3, BC); MF16(a1[3], F3, CC); \
      CV_B(BN, c0 + 32 * (k) + 32, 0); CV_B(CN, c0 + 32 * (k) + 32, 1); DS_B128(F3.u, aad, 256 + 64 * (k)); SCHED(); \
      MF16(a0[2], F2, BC); MF16(a1[2], F2, CC); MF16(a0[1], F1, BC); MF16(a1[1], F1, CC); \
      WAIT_LGKM(3); SCHED(); MF16(a0[0], F0, BC); MF16(a1[0], F0, CC); SCHED();
#pragma unroll 1
      for (int it = 0; it < 3; ++it) {
        CV_STEP2(W1, W2, W3, W0, B0, B2, B1, B3, 0)
        CV_STEP2(W2, W3, W0, W1, B1, B3, B0, B2, 1)
        CV_STEP2(W3, W0, W1, W2, B0, B2, B1, B3, 2)
        CV_STEP2(W0, W1, W2, W3, B1, B3, B0, B2, 3)
        c0 += 128; aad += 256;
        if (it == 0 && has_tile) {
          WAIT_VM(0);
          if (lane == 0) set_flag(hflag, ep);
        }
      }
      WAIT_LGKM(0); SCHED();
      MF16(a0[3], W1, B0); MF16(a1[3], W1, B2);
      MFMA_DRAIN();
      SCHED();
    }

    LAS float* xch = (LAS float*)(smem + CV_XCH + ch * 12288);
    auto put = [&](int slot, const f32x4& p0, const f32x4& p1) {
      *(LAS f32x4*)(xch + slot * 512 + lane * 4) = p0; *(LAS f32x4*)(xch + slot * 512 + 256 + lane * 4) = p1; };
    if (v4 == 0) put(0, a1[2], a1[3]);
    else if (v4 == 1) put(1, a1[0], a1[1]);
    else if (v4 == 2) put(4, a1[2], a1[3]);
    else { put(2, a1[0], a1[1]); put(3, a1[2], a1[3]); put(5, a0[0], a0[1]); }
    BARRIER();

    f32x4 y0, y1;
    {
      const int s1 = v4 == 0 ? 1 : (v4 == 1 ? 0 : (v4 == 2 ? 5 : 4));
      y0 = v4 == 3 ? a0[2] : ((v4 & 1) ? a1[2] : a1[0]);
      y1 = v4 == 3 ? a0[3] : ((v4 & 1) ? a1[3] : a1[1]);
      y0 += *(LAS f32x4*)(xch + s1 * 512 + lane * 4); y1 += *(LAS f32x4*)(xch + s1 * 512 + 256 + lane * 4);
      if (v4 < 2) { y0 += *(LAS f32x4*)(xch + (2 + v4) * 512 + lane * 4); y1 += *(LAS f32x4*)(xch + (2 + v4) * 512 + 256 + lane * 4); }
    }
    const float ki = *(LAS float*)cbase;
    const float dk = bf2f(A.dsk[d]);
    const int Tf = v4 < 2 ? 1 : 0, t0 = 2 * (v4 & 1);
    const int l1 = 8 * Tf + (r >> 1), sft = r & 1;
    const __amdgpu_buffer_rsrc_t rs = __builtin_amdgcn_make_buffer_rsrc(A.sT, 0, D_ * L_ * 2, 0x00020000);
#pragma unroll
    for (int tt = 0; tt < 2; ++tt) {
      const f32x4 y = tt ? y1 : y0;
      const float send0 = sft ? y[0] : y[2], send1 = sft ? y[1] : y[3];
      const float rcv0 = __builtin_bit_cast(float, __builtin_amdgcn_update_dpp(0, __builtin_bit_cast(int, send0), 0xB1, 0xf, 0xf, true));
      const float rcv1 = __builtin_bit_cast(float, __builtin_amdgcn_update_dpp(0, __builtin_bit_cast(int, send1), 0xB1, 0xf, 0xf, true));
      float z[4];
      z[0] = sft ? rcv0 : y[0]; z[1] = sft ? y[2] : rcv0; z[2] = sft ? rcv1 : y[1]; z[3] = sft ? y[3] : rcv1;
      const int l = 128 * l1 + 32 * (t0 + tt) + 8 * g + 4 * sft;
      const int c = l >> 3;
      const u32x2 uu = *(LAS u32x2*)(cbase + UB0 + 16 * (c + 2 * (c >> 4) + 4) + 8 * sft);
      float o[4];
#pragma unroll
      for (int i = 0; i < 4; ++i) {
        const unsigned hw = (i & 1) ? (uu[i >> 1] >> 16) : (uu[i >> 1] & 0xffffu);
        const float uf = (float)__builtin_bit_cast(_Float16, (u16)hw);
        o[i] = bfr(z[i] * ki) + bfr(uf * dk);
      }
      __builtin_amdgcn_raw_buffer_store_b64((u32x2){pkbf(o[0], o[1]), pkbf(o[2], o[3])}, rs, (unsigned)(d * L_ + l) * 2u, 0, 16);
    }

    BARRIER();
  }

  constexpr int NST = F_ / 64;
  u64* sflag = A.fl + FL_S + b * 8 + w;
  if (w >= 4) {
    const int j = w - 4;
    const char* abase = (const char*)(A.h + (size_t)g2m0 * HP_);
    unsigned aoff[2];
#pragma unroll
    for (int i = 0; i < 2; ++i) {
      const int r = 8 * (2 * j + i) + (lane >> 3);
      aoff[i] = (unsigned)(r * HP_ + 8 * ((lane & 7) ^ ((r >> 1) & 7))) * 2u;
    }
    const char* ap = abase;
    unsigned slot = 0;
#define G2_LDA() do { glds16_sc1(ap + aoff[0], smem + slot + (2 * j) * 1024); glds16_sc1(ap + aoff[1], smem + slot + (2 * j + 1) * 1024); \
                      ap += 128; slot = (slot == (G2_NA - 1) * 8192) ? 0u : slot + 8192; } while (0)
#pragma unroll 1
    for (int s = 0; s < 8; ++s) G2_LDA();
    WAIT_VM(16);
    if (lane == 0) set_flag(sflag, ep);
    WAIT_VM(15);
    BARRIER();
#pragma unroll 1
    for (int i = 0; i < 10; ++i) { WAIT_VM(10); BARRIER(); G2_LDA(); G2_LDA(); }
    WAIT_VM(10); BARRIER();
    WAIT_VM(6); BARRIER();
    WAIT_VM(2); BARRIER();
    WAIT_VM(0); BARRIER();
    return;
  }
  {
    const int wm = w >> 1, wn = w & 1;
    const int mrow = 32 * wm + (lane & 31), hh = lane >> 5;
    unsigned xoff[4];
#pragma unroll
    for (int ks = 0; ks < 4; ++ks) xoff[ks] = sbase + mrow * 128 + (((2 * ks + hh) ^ ((mrow >> 1) & 7)) << 4);
    const int g4 = (lane >> 4) & 1, q = (lane & 15) >> 2, p = lane & 3;
    const unsigned woff = sbase + G2_BR + (8 * hh + q) * 128 + (((2 * wn + (p & 1)) ^ (q & 2)) * 32) + 8 * (2 * g4 + (p >> 1));
    f32x16 acc = {};
    struct G2Frags { Frag x[4]; Frag w[4]; } FA, FB;
#define G2_RD(S, sa, sb) do { _Pragma("unroll") for (int ks = 0; ks < 4; ++ks) { \
      DS_B128(S.x[ks].u, xoff[ks] + (sa), 0); DS_TR(S.w[ks].s.a, woff + (sb), ks * 2048); DS_TR(S.w[ks].s.b, woff + (sb), ks * 2048 + 512); } } while (0)
#define G2_MM(S) do { _Pragma("unroll") for (int ks = 0; ks < 4; ++ks) MFMA32_BF16(acc, S.w[ks].u, S.x[ks].u); } while (0)
#define G2_RD1(S, ks, sa, sb) do { DS_B128(S.x[ks].u, xoff[ks] + (sa), 0); DS_TR(S.w[ks].s.a, woff + (sb), ks * 2048); DS_TR(S.w[ks].s.b, woff + (sb), ks * 2048 + 512); } while (0)
#define G2_BAR() do { asm volatile("" ::: "memory"); __builtin_amdgcn_s_barrier(); asm volatile("" ::: "memory"); SCHED(); } while (0)
#define G2_IT(CUR, NXT) do { \
    WAIT_LGKM(9); SCHED(); MFMA32_BF16(acc, CUR.w[0].u, CUR.x[0].u); G2_RD1(NXT, 0, an, bn); SCHED(); \
    WAIT_LGKM(9); SCHED(); MFMA32_BF16(acc, CUR.w[1].u, CUR.x[1].u); G2_RD1(NXT, 1, an, bn); SCHED(); \
    WAIT_LGKM(9); SCHED(); MFMA32_BF16(acc, CUR.w[2].u, CUR.x[2].u); G2_RD1(NXT, 2, an, bn); SCHED(); \
    WAIT_LGKM(9); SCHED(); MFMA32_BF16(acc, CUR.w[3].u, CUR.x[3].u); G2_RD1(NXT, 3, an, bn); SCHED(); \
    an = (an == (G2_NA - 1) * 8192) ? 0u : an + 8192; bn = (bn == (G2_NB - 1) * 8192) ? 0u : bn + 8192; } while (0)
    const size_t go = (size_t)(g2m0 + mrow) * D_ + g2n0 + 32 * wn + 16 * hh;
    u32x4 xa, xb;
    asm volatile("global_load_dwordx4 %0, %2, off\n\tglobal_load_dwordx4 %1, %2, off offset:16" : "=&v"(xa), "=&v"(xb) : "v"(A.x + go) : "memory");
    WAIT_VM(2);
    if (lane == 0) set_flag(sflag, ep);
    BARRIER(); SCHED();

    G2_RD(FA, 0u, 0u);
    SCHED();
    unsigned an = 8192, bn = 8192;
#pragma unroll 1
    for (int i = 0; i < 13; ++i) { G2_BAR(); G2_IT(FA, FB); G2_IT(FB, FA); }
    G2_BAR();
    G2_IT(FA, FB);
    WAIT_LGKM(0); SCHED();
    G2_MM(FB);
    MFMA_DRAIN();
    SCHED();

    asm volatile("s_waitcnt vmcnt(0)" : "+v"(xa), "+v"(xb) :: "memory");
    BARRIER();
    float o[16];
#pragma unroll
    for (int a = 0; a < 4; ++a) {
      const s16x4 sv = trread(smem + ST_OFF + (32 * wn + 16 * hh + 4 * a + q) * 144 + (32 * wm + 16 * g4 + 4 * p) * 2);
#pragma unroll
      for (int i = 0; i < 4; ++i) {
        const int e = 4 * a + i;
        const float ov = bfr(acc[e]);
        const float gv = bfr(ov * __builtin_amdgcn_rcpf(1.0f + __builtin_amdgcn_exp2f(-1.4426950408889634f * ov)));
        const float sf = bf2f((unsigned)(u16)sv[i]);
        const unsigned xw = e < 8 ? xa[e >> 1] : xb[(e - 8) >> 1];
        const float xf = (e & 1) ? bfhi(xw) : bflo(xw);
        o[e] = xf + bfr(sf * gv);
      }
    }
    u32x4 va, vb;
#pragma unroll
    for (int j = 0; j < 4; ++j) { va[j] = pkbf(o[2 * j], o[2 * j + 1]); vb[j] = pkbf(o[8 + 2 * j], o[9 + 2 * j]); }
    *(u32x4*)(A.out + go) = va;
    *(u32x4*)(A.out + go + 8) = vb;

  }
}

#if defined(__HIP_DEVICE_COMPILE__)
#pragma clang attribute pop
#endif

extern "C" void kernel_launch(void* const* d_in, const int* in_sizes, int n_in, void* d_out, int out_size,
                              void* d_ws, size_t ws_size, hipStream_t stream) {
  char* ws = (char*)d_ws;
  KFArgs a;
  a.x = (const u16*)d_in[0]; a.lAr = (const u16*)d_in[1]; a.Aim = (const u16*)d_in[2]; a.Cr = (const u16*)d_in[3];
  a.Ci = (const u16*)d_in[4]; a.ldt = (const u16*)d_in[5]; a.dsk = (const u16*)d_in[6];
  a.wg = (const u16*)d_in[8]; a.wu = (const u16*)d_in[9]; a.wd = (const u16*)d_in[10];
  a.uT = (u16*)(ws + OFF_UT);
  a.h = (u16*)(ws + OFF_H); a.sT = (u16*)(ws + OFF_ST); a.fl = (u64*)(ws + OFF_FL); a.out = (u16*)d_out;
  (void)hipFuncSetAttribute((const void*)kf_main, hipFuncAttributeMaxDynamicSharedMemorySize, KF_LDS);
  kf_main<<<256, 1024, KF_LDS, stream>>>(a);
}
```
